```python
import torch

def _softmax(x, axis=-1):
    return torch.softmax(x, dim=axis)
def _gelu(x):
    return torch.nn.functional.gelu(x, approximate="tanh")
def _silu(x):
    return torch.nn.functional.silu(x)
def _sigmoid(x):
    return torch.sigmoid(x)
def _layernorm(x, axis=-1, eps=1e-5):
    m = x.mean(dim=axis, keepdim=True)
    v = x.var(dim=axis, keepdim=True, unbiased=False)
    return (x - m) / torch.sqrt(v + eps)
def _scatter_add(tgt, idx, src, axis=0):
    return tgt.clone().index_add_(axis, idx, src)
def _segment_sum(src, seg, n):
    out = torch.zeros((n,) + src.shape[1:], dtype=src.dtype)
    return out.index_add_(0, seg, src)
def _pad(x, pw):
    flat = tuple(v for pair in reversed(pw) for v in pair)
    return torch.nn.functional.pad(x, flat)

kernel_name = 'standalone__edge_featured_mpnn_gatedgcn__graph'

def setup_inputs():

    _w = (lambda a: (lambda t: t.to(torch.bfloat16) if t.is_floating_point() else t)(torch.as_tensor(a)))
    N, deg, D, De = 25000, 16, 128, max(16, 1)
    E = N * deg
    return {
        "x": _w((lambda *s: torch.randn(*s, dtype=torch.bfloat16) * 0.4)(N, D)),
        "e_attr": _w((lambda *s: torch.randn(*s, dtype=torch.bfloat16) * 0.4)(E, De)),
        "src": _w((lambda lo, hi, *s: torch.randint(lo, hi, s))(0, N, E).to(torch.int64)),
        "dst": _w((lambda lo, hi, *s: torch.randint(lo, hi, s))(0, N, E).to(torch.int64)),
        "w_a": _w((lambda *s: torch.randn(*s, dtype=torch.bfloat16) * 0.4)(D, D)),
        "w_b": _w((lambda *s: torch.randn(*s, dtype=torch.bfloat16) * 0.4)(D, D)),
        "w_c": _w((lambda *s: torch.randn(*s, dtype=torch.bfloat16) * 0.4)(De, D)),
        "w_u": _w((lambda *s: torch.randn(*s, dtype=torch.bfloat16) * 0.05)(D, D)),
        "w_v": _w((lambda *s: torch.randn(*s, dtype=torch.bfloat16) * 0.05)(D, D)),
    }

def reference(x, e_attr, src, dst, w_a, w_b, w_c, w_u, w_v):
    N, D = x.shape
    hv = x @ w_v
    g = x[dst] @ w_a + x[src] @ w_b + e_attr @ w_c
    eta = _sigmoid(g)
    denom = _segment_sum(eta, dst, N) + 1e-6
    eta_n = eta / denom[dst]
    agg = _segment_sum(eta_n * hv[src], dst, N)
    z = x @ w_u + agg
    relu_z = torch.where(z > 0, z, 0 * z)
    return x + relu_z
```

```cpp
#include <hip/hip_runtime.h>
#include <hip/hip_bf16.h>
#include <stdint.h>

static __device__ __forceinline__ float bf2f(unsigned short u) { return __uint_as_float(((unsigned)u) << 16); }
static __device__ __forceinline__ unsigned short f2bf(float f) { __hip_bfloat16 b = __float2bfloat16(f); return *(unsigned short*)&b; }

__global__ void k_gemm(const unsigned short* x, const unsigned short* wa, const unsigned short* wb,
                       const unsigned short* wu, const unsigned short* wv, float* T, float* num, float* den, int N) {
    int n = blockIdx.x; int c = threadIdx.x;
    __shared__ float xs[128];
    xs[c] = bf2f(x[n * 128 + c]);
    __syncthreads();
    float a = 0, b = 0, u = 0, v = 0;
    for (int k = 0; k < 128; ++k) {
        float xv = xs[k];
        a += xv * bf2f(wa[k * 128 + c]);
        b += xv * bf2f(wb[k * 128 + c]);
        u += xv * bf2f(wu[k * 128 + c]);
        v += xv * bf2f(wv[k * 128 + c]);
    }
    size_t S = (size_t)N * 128;
    T[n * 128 + c] = a; T[S + n * 128 + c] = b; T[2 * S + n * 128 + c] = u; T[3 * S + n * 128 + c] = v;
    num[n * 128 + c] = 0; den[n * 128 + c] = 0;
}
__global__ void k_edge(const unsigned short* ea, const int* src, const int* dst, const unsigned short* wc,
                       const float* T, float* num, float* den, int N) {
    int e = blockIdx.x; int c = threadIdx.x;
    size_t S = (size_t)N * 128;
    int s = src[e], d = dst[e];
    float g = T[d * 128 + c] + T[S + s * 128 + c];
    for (int k = 0; k < 16; ++k) g += bf2f(ea[e * 16 + k]) * bf2f(wc[k * 128 + c]);
    float eta = 1.f / (1.f + __expf(-g));
    atomicAdd(&num[d * 128 + c], eta * T[3 * S + s * 128 + c]);
    atomicAdd(&den[d * 128 + c], eta);
}
__global__ void k_fin(const unsigned short* x, const float* T, const float* num, const float* den, unsigned short* out, int N) {
    int i = blockIdx.x * blockDim.x + threadIdx.x;
    size_t S = (size_t)N * 128;
    if (i >= N * 128) return;
    float z = T[2 * S + i] + num[i] / (den[i] + 1e-6f);
    out[i] = f2bf(bf2f(x[i]) + (z > 0 ? z : 0.f));
}

extern "C" void kernel_launch(void* const* d_in, const int* in_sizes, int n_in,
                              void* d_out, int out_size, void* d_ws, size_t ws_size,
                              hipStream_t stream) {
    const unsigned short* x = (const unsigned short*)d_in[0];
    const unsigned short* ea = (const unsigned short*)d_in[1];
    const int* src = (const int*)d_in[2];
    const int* dst = (const int*)d_in[3];
    const unsigned short* wa = (const unsigned short*)d_in[4];
    const unsigned short* wb = (const unsigned short*)d_in[5];
    const unsigned short* wc = (const unsigned short*)d_in[6];
    const unsigned short* wu = (const unsigned short*)d_in[7];
    const unsigned short* wv = (const unsigned short*)d_in[8];
    int N = in_sizes[0] / 128; int E = in_sizes[2];
    float* T = (float*)d_ws; float* num = T + (size_t)4 * N * 128; float* den = num + (size_t)N * 128;
    k_gemm<<<N, 128, 0, stream>>>(x, wa, wb, wu, wv, T, num, den, N);
    k_edge<<<E, 128, 0, stream>>>(ea, src, dst, wc, T, num, den, N);
    k_fin<<<(N * 128 + 255) / 256, 256, 0, stream>>>(x, T, num, den, (unsigned short*)d_out, N);
}
```

```cpp
#include <hip/hip_runtime.h>
#include <stdint.h>

typedef unsigned u32;
typedef unsigned long long u64;
typedef unsigned short u16;
typedef float f32x16 __attribute__((ext_vector_type(16)));
typedef float f32x8 __attribute__((ext_vector_type(8)));
typedef float f32x2 __attribute__((ext_vector_type(2)));
typedef __bf16 bf16x2 __attribute__((ext_vector_type(2)));
typedef __bf16 bf16x8 __attribute__((ext_vector_type(8)));
typedef __bf16 bf16x16 __attribute__((ext_vector_type(16)));
typedef short s16x4 __attribute__((ext_vector_type(4)));
typedef u32 u32x2 __attribute__((ext_vector_type(2)));
typedef u32 u32x4 __attribute__((ext_vector_type(4)));
typedef int i32x4 __attribute__((ext_vector_type(4)));
typedef u32 u32x4u __attribute__((ext_vector_type(4), aligned(4)));
#define LAS __attribute__((address_space(3)))
#define DI __device__ __forceinline__

constexpr int NN = 25000, NE = 400000, NB = 98, CH = 1600, NCH = 250;
constexpr int XP = 1088;
constexpr u32 TAB_BYTES = 13u << 21;
constexpr u32 REC_OFF = TAB_BYTES;
constexpr u32 OFF_OFF = REC_OFF + 1600512;
constexpr u32 POL_OFF = OFF_OFF + 256000;
constexpr u32 VFL_OFF = POL_OFF + 1024;
constexpr float NLOG2E = -1.4426950408889634f;

#define ST(k, i)
#define STW(k, i, wv)

DI char* tab_base(char* ws) { return (char*)(((u64)ws + 4096 + 0x1fffffull) & ~0x1fffffull); }
template <class T> DI T lds_ld(u32 a) { return *(const LAS T*)a; }
template <class T> DI void lds_st(u32 a, T v) { *(LAS T*)a = v; }
DI u32 lds_add(u32 a, u32 v) { return __hip_atomic_fetch_add((LAS u32*)a, v, __ATOMIC_RELAXED, __HIP_MEMORY_SCOPE_WORKGROUP); }
DI void lds_or(u32 a, u32 v) { (void)__hip_atomic_fetch_or((LAS u32*)a, v, __ATOMIC_RELAXED, __HIP_MEMORY_SCOPE_WORKGROUP); }
DI s16x4 lds_tr(u32 a) { return __builtin_amdgcn_ds_read_tr16_b64_v4i16((LAS s16x4*)a); }
DI bf16x8 lds_tr2(u32 a0, u32 a1) {
    s16x4 lo = lds_tr(a0), hi = lds_tr(a1);
    return __builtin_bit_cast(bf16x8, __builtin_shufflevector(lo, hi, 0, 1, 2, 3, 4, 5, 6, 7));
}
DI void glds16(const void* g, u32 dst) {
    u32 keep;
    asm volatile("s_mov_b32 %0, m0\n\ts_mov_b32 m0, %2\n\ts_nop 0\n\tglobal_load_lds_dwordx4 %1, off\n\ts_mov_b32 m0, %0"
                 : "=&s"(keep) : "v"(g), "s"(dst) : "memory");
}
DI void wg_barrier() { asm volatile("s_waitcnt lgkmcnt(0)\n\ts_barrier" ::: "memory"); }
DI u32 pk_bf16(float a, float b) { f32x2 v = {a, b}; return __builtin_bit_cast(u32, __builtin_convertvector(v, bf16x2)); }
DI float bf_lo(u32 v) { return __uint_as_float(v << 16); }
DI float bf_hi(u32 v) { return __uint_as_float(v & 0xffff0000u); }
DI float bf2f(u16 v) { return __uint_as_float((u32)v << 16); }
#define MFMA(a, b, c) __builtin_amdgcn_mfma_f32_32x32x16_bf16((a), (b), (c), 0, 0, 0)
template <int CTRL> DI u32 dpp0(u32 v) { return __builtin_amdgcn_update_dpp(0u, v, CTRL, 0xf, 0xf, true); }
DI u32 row_scan(u32 v) { v += dpp0<0x111>(v); v += dpp0<0x112>(v); v += dpp0<0x114>(v); v += dpp0<0x118>(v); return v; }
DI u32 xcc_id() { return (u32)__builtin_amdgcn_s_getreg((3 << 11) | 20) & 0xfu; }

constexpr u32 K1_W = 0, K1_X = 65536, K1_HIST = K1_X + 32 * XP, K1_BASE = K1_HIST + 1024, K1_CNT = K1_BASE + 1024, K1_LDS = K1_CNT + 16;

__global__ __launch_bounds__(1024) void k1(const u16* __restrict__ x, const int* __restrict__ src, const int* __restrict__ dst,
                                           const u16* __restrict__ wb, const u16* __restrict__ wv, char* __restrict__ ws) {
    __shared__ __attribute__((aligned(16))) char smem[K1_LDS];
    const u32 sm = (u32)(size_t)smem;
    const int tid = threadIdx.x, lane = tid & 63, w = __builtin_amdgcn_readfirstlane(tid >> 6);
    const int j = blockIdx.x, cls = j & 7, li = j >> 3;
    char* tab = tab_base(ws);
    ST(0, 0);
    const int extra = (cls < 7 && li < 2) ? 1 : 0;
    const int tile0 = cls * 98 + li * 3 + (cls < 7 ? (li < 2 ? li : 2) : 0), ntile = 3 + extra;
    if (tid < 256) lds_st<u32>(sm + K1_HIST + tid * 4, 0u);
    if (tid < 4) lds_st<u32>(sm + K1_CNT + tid * 4, 0u);

    if (w == 15) {
        wg_barrier();
        if (lane == 0) {
            u32* pa = (u32*)(tab + ((u64)(li % 13) << 21) + (u32)((li / 13) * 8 + cls) * 16384u);
            u32* pb = pa + 2048;
            u32 ba = 0xffffffffu, bb = 0xffffffffu, z = 0, v;
            for (int r = 0; r < 4; ++r) {
                u64 t0, t1, t2;
                asm volatile("s_memtime %0\n\ts_waitcnt lgkmcnt(0)\n\tglobal_atomic_add %3, %4, %6, off sc0\n\ts_waitcnt vmcnt(0)\n\t"
                             "s_memtime %1\n\ts_waitcnt lgkmcnt(0)\n\tglobal_atomic_add %3, %5, %6, off sc0\n\ts_waitcnt vmcnt(0)\n\t"
                             "s_memtime %2\n\ts_waitcnt lgkmcnt(0)"
                             : "=&s"(t0), "=&s"(t1), "=&s"(t2), "=&v"(v) : "v"(pa), "v"(pb), "v"(z) : "memory");
                ba = min(ba, (u32)(t1 - t0)); bb = min(bb, (u32)(t2 - t1));
            }
            int d = (int)ba - (int)bb; d = max(-400, min(400, d));
            ((int*)(tab + POL_OFF))[j] = d * 16 + (int)xcc_id();
        }
        STW(0, 5, 15);
        return;
    }

    const int pt = tid - 512;
    const bool pact = (w >= 8) && (pt < 400) && (j < NCH);
    i32x4 dv = {0, 0, 0, 0}, sv = {0, 0, 0, 0};
    if (pact) { dv = *(const i32x4*)(dst + j * CH + pt * 4); sv = *(const i32x4*)(src + j * CH + pt * 4); }
    {
        const int q = lane >> 4, c = lane & 15, nx = ntile * 8;
        for (int p = w; p < nx + 64; p += 15) {
            if (p < nx) {
                int row = min(tile0 * 32 + p * 4 + q, NN - 1);
                glds16(x + row * 128 + ((c ^ q) << 3), sm + K1_X + p * XP);
            } else {
                int pw = p - nx; const u16* wp = pw < 32 ? wb : wv;
                glds16(wp + ((pw & 31) * 4 + q) * 128 + ((c ^ (q << 2)) << 3), sm + K1_W + pw * 1024);
            }
        }
    }
    wg_barrier();
    asm volatile("s_waitcnt vmcnt(0)" ::: "memory");
    if (lane == 0) lds_add(sm + K1_CNT, 1u);

    if (w < 8) {
        while (lds_ld<volatile u32>(sm + K1_CNT) < 15u) __builtin_amdgcn_s_sleep(1);
        asm volatile("" ::: "memory");
        ST(0, 1);
        const int m = w & 3, r = lane & 31, h = lane >> 5, q = (lane >> 2) & 3, p = lane & 3, cb = (lane >> 4) & 1;
        const u32 wad = sm + K1_W + (8 * h + q) * 256 + (((4 * m + 2 * cb + (p >> 1)) ^ (q << 2)) << 4) + (p & 1) * 8;
        const u32 xa0 = sm + K1_X + (r >> 2) * XP + (r & 3) * 256 + ((h ^ (r & 3)) << 4);
        const u32 xa1 = sm + K1_X + (r >> 2) * XP + (r & 3) * 256 + (((2 + h) ^ (r & 3)) << 4);
        const auto rs = __builtin_amdgcn_make_buffer_rsrc((void*)tab, 0, TAB_BYTES, 0x00020000);
        const u32 voff = h * 2048 + m * 128 + r * 4;
        float vmax = 0.f;
        for (int t = w >> 2; t < ntile; t += 2) {
            f32x16 ab = {}, av = {};
#pragma unroll
            for (int s = 0; s < 8; ++s) {
                bf16x8 xf = lds_ld<bf16x8>(((s & 1) ? xa1 : xa0) + t * 8 * XP + (s >> 1) * 64);
                bf16x8 fb = lds_tr2(wad + s * 4096, wad + s * 4096 + 1024);
                bf16x8 fv = lds_tr2(wad + 32768 + s * 4096, wad + 32768 + s * 4096 + 1024);
                ab = MFMA(xf, fb, ab); av = MFMA(xf, fv, av);
            }
            const u32 so = (u32)(tile0 + t) * 32768u;
#pragma unroll
            for (int i = 0; i < 16; ++i) {
                vmax = fmaxf(vmax, fabsf(av[i]));
                const u32 v16 = (u32)__builtin_bit_cast(u16, (_Float16)av[i]);
                const u32 bits = __float_as_uint(__builtin_fmaf(ab[i], NLOG2E, 0x1p-100f));
                const u32 d = ((bits + 0x8000u - v16) & 0xffff0000u) | v16;
                const u32 sb = so + (i >> 3) * 16384 + ((i >> 2) & 1) * 4096;
                __builtin_amdgcn_raw_buffer_store_b32(d, rs, voff + (i & 3) * 512, sb, 0);
                __builtin_amdgcn_raw_buffer_store_b32(d, rs, voff + (i & 3) * 512, sb + 8192, 0);
            }
        }
        const u32 fl = (__any(!(vmax <= 60000.f)) ? 1u : 0u) | (__any(vmax >= 0x1p-10f) ? 2u : 0u) | (__any(vmax > 0.f) ? 4u : 0u);
        if (lane == 0) ((unsigned char*)(tab + VFL_OFF))[j * 8 + w] = (unsigned char)fl;
        ST(0, 2);
    } else if (j < NCH) {
        u32 b[4], rk[4];
#pragma unroll
        for (int k = 0; k < 4; ++k) { b[k] = ((u32)dv[k] * 42800u) >> 22; rk[k] = 0; }
        if (pact) {
#pragma unroll
            for (int k = 0; k < 4; ++k) rk[k] = lds_add(sm + K1_HIST + b[k] * 4, 1u);
        }
        asm volatile("s_waitcnt lgkmcnt(0)" ::: "memory");
        if (lane == 0) lds_add(sm + K1_CNT + 4, 1u);
        while (lds_ld<volatile u32>(sm + K1_CNT + 4) < 7u) __builtin_amdgcn_s_sleep(1);
        asm volatile("" ::: "memory");
        const u32x4 c = lds_ld<u32x4>(sm + K1_HIST + lane * 16);
        const u32 tot = c[0] + c[1] + c[2] + c[3];
        u32 inc = row_scan(tot);
        const u32 r0 = __builtin_amdgcn_readlane(inc, 15), r1 = __builtin_amdgcn_readlane(inc, 31), r2 = __builtin_amdgcn_readlane(inc, 47);
        inc += (lane >= 16 ? r0 : 0) + (lane >= 32 ? r1 : 0) + (lane >= 48 ? r2 : 0);
        u32x4 st; st[0] = inc - tot; st[1] = st[0] + c[0]; st[2] = st[1] + c[1]; st[3] = st[2] + c[2];
        lds_st<u32x4>(sm + K1_BASE + lane * 16, st);
        if (w == 8) *(u32x4*)(tab + OFF_OFF + j * 1024 + lane * 16) = st | (c << 16);
        asm volatile("s_waitcnt lgkmcnt(0)" ::: "memory");
        if (pact) {
            u32* rec = (u32*)(tab + REC_OFF) + j * CH;
#pragma unroll
            for (int k = 0; k < 4; ++k) {
                const u32 pos = lds_ld<volatile u32>(sm + K1_BASE + b[k] * 4) + rk[k];
                rec[pos] = ((u32)sv[k] * NB + ((u32)dv[k] - b[k] * NB)) * CH + (u32)(pt * 4 + k);
            }
        }
        STW(0, 4, 8);
    }
}

constexpr u32 K2_W = 0;
constexpr u32 K2_WC = 65536;
constexpr u32 K2_X = K2_WC + 4096;
constexpr u32 K2_TOFF = K2_X + 32 * XP;
constexpr u32 K2_EOFF = K2_TOFF + 8192;
constexpr u32 K2_DLT = K2_EOFF + 8192;
constexpr u32 K2_DLS = K2_DLT + 4096;
constexpr u32 K2_LUTT = K2_DLS + 4096;
constexpr u32 K2_LUTS = K2_LUTT + 26 * 256;
constexpr u32 K2_HIST = K2_LUTS + 26 * 80;
constexpr u32 K2_BASE = K2_HIST + 512;
constexpr u32 K2_POL = K2_BASE + 512;
constexpr u32 K2_LDS = K2_POL + 64;
constexpr int LA = 3;

__device__ __noinline__ void slow_bucket(const u16* x, const u16* ea, const u16* wa, const u16* wb, const u16* wc, const u16* wu,
                                         const u16* wv, const char* tab, u16* out, int bucket, u32 sm) {
    const int tid = threadIdx.x, c = tid & 127, sub = tid >> 7, node0 = bucket * NB;
    __syncthreads();
    for (int i = tid; i < NB * 256; i += 1024) lds_st<float>(sm + i * 4, 0.f);
    __syncthreads();
    for (int cell = sub; cell < NCH; cell += 8) {
        const u32 oc = ((const u32*)(tab + OFF_OFF))[cell * 256 + bucket];
        for (u32 k = 0; k < (oc >> 16); ++k) {
            const u32 v = ((const u32*)(tab + REC_OFF))[cell * CH + (oc & 0xffffu) + k];
            const u32 qd = v / CH, el = v - qd * CH, s = qd / NB, dl = qd - s * NB, e = cell * CH + el;
            float g = 0.f, hv = 0.f;
            for (int kk = 0; kk < 128; ++kk) {
                const float xs = bf2f(x[s * 128 + kk]);
                g += xs * bf2f(wb[kk * 128 + c]) + bf2f(x[(node0 + dl) * 128 + kk]) * bf2f(wa[kk * 128 + c]);
                hv += xs * bf2f(wv[kk * 128 + c]);
            }
            for (int kk = 0; kk < 16; ++kk) g += bf2f(ea[e * 16 + kk]) * bf2f(wc[kk * 128 + c]);
            const float eta = 1.f / (1.f + __expf(-g));
            __hip_atomic_fetch_add((LAS float*)(sm + (dl * 256 + c) * 4), eta * hv, __ATOMIC_RELAXED, __HIP_MEMORY_SCOPE_WORKGROUP);
            __hip_atomic_fetch_add((LAS float*)(sm + (dl * 256 + 128 + c) * 4), eta, __ATOMIC_RELAXED, __HIP_MEMORY_SCOPE_WORKGROUP);
        }
    }
    __syncthreads();
    for (int dl = sub; dl < NB && node0 + dl < NN; dl += 8) {
        float u = 0.f;
        for (int kk = 0; kk < 128; ++kk) u += bf2f(x[(node0 + dl) * 128 + kk]) * bf2f(wu[kk * 128 + c]);
        const float z = u + lds_ld<float>(sm + (dl * 256 + c) * 4) / (lds_ld<float>(sm + (dl * 256 + 128 + c) * 4) + 1e-6f);
        out[(node0 + dl) * 128 + c] = (u16)pk_bf16(bf2f(x[(node0 + dl) * 128 + c]) + fmaxf(z, 0.f), 0.f);
    }
}

__global__ __launch_bounds__(1024) void k2(const u16* __restrict__ x, const u16* __restrict__ ea, const u16* __restrict__ wa,
                                           const u16* __restrict__ wb, const u16* __restrict__ wc, const u16* __restrict__ wu,
                                           const u16* __restrict__ wv, char* __restrict__ ws, u16* __restrict__ out) {
    __shared__ __attribute__((aligned(16))) char smem[K2_LDS];
    const u32 sm = (u32)(size_t)smem;
    const int tid = threadIdx.x, lane = tid & 63, w = __builtin_amdgcn_readfirstlane(tid >> 6);
    const int j = blockIdx.x, bucket = (j & 7) * 32 + (j >> 3), node0 = bucket * NB;
    const int g = w >> 2, m = w & 3, r = lane & 31, h = lane >> 5;
    const char* tab = tab_base(ws);
    ST(1, 0);

    const int cell = min(tid >> 2, NCH - 1), q4 = tid & 3;
    u32 oc;
    {
        const u32* p = (const u32*)(tab + OFF_OFF) + cell * 256 + bucket;
        asm volatile("global_load_dword %0, %1, off" : "=v"(oc) : "v"(p) : "memory");
        const int q = lane >> 4, c = lane & 15;
#pragma unroll
        for (int a = 0; a < 2; ++a) {
            const int p2 = 2 * w + a, row = min(node0 + 4 * (4 * (p2 & 7) + q) + g, NN - 1);
            glds16(x + row * 128 + ((c ^ q) << 3), sm + K2_X + p2 * XP);
        }
        const u16* wp = (w < 8 ? wa : wu) + ((w & 7) * 16 + q) * 128 + ((c ^ (q << 2)) << 3);
#pragma unroll
        for (int a = 0; a < 4; ++a) glds16(wp + a * 512, sm + K2_W + (w * 4 + a) * 1024);
        glds16(wc + (m * 4 + q) * 128 + ((c ^ (q << 2)) << 3), sm + K2_WC + m * 1024);
    }
    {
        const u32 pad = tid * 16;
        lds_st<u32x4>(sm + K2_TOFF + pad, tid < 512 ? u32x4{128u, 128u, 128u, 128u} : u32x4{0u, 0u, 0u, 0u});
        if (tid < 512) { const u32 v = tid < 256 ? (25u * 256u) * 0x10001u : (25u * 80u) * 0x10001u; lds_st<u32x4>(sm + K2_DLT + pad, u32x4{v, v, v, v}); }
        else if (tid < 512 + 416) {
            const int e = tid - 512, row = e >> 4, col0 = (e & 3) * 8, d = row - col0;
            u32x4 v = {0u, 0u, 0u, 0u};
            if (row < 25 && d >= 0 && d < 8) { const u32 o = (d & 1) ? 0x3f800000u : 0x3f80u; v[0] = (d >> 1) == 0 ? o : 0u; v[1] = (d >> 1) == 1 ? o : 0u; v[2] = (d >> 1) == 2 ? o : 0u; v[3] = (d >> 1) == 3 ? o : 0u; }
            lds_st<u32x4>(sm + K2_LUTT + e * 16, v);
        }
        if (tid < 104) {
            const int row = tid >> 2, sh = tid & 3, d = ((row >> 3) & 1) * 4 + (row & 3);
            const bool on = row < 25 && (row >> 4) == (sh >> 1) && ((row >> 2) & 1) == (sh & 1);
            const u32 o = on ? ((d & 1) ? 0x3f800000u : 0x3f80u) : 0u;
            u32x4 v; v[0] = (d >> 1) == 0 ? o : 0u; v[1] = (d >> 1) == 1 ? o : 0u; v[2] = (d >> 1) == 2 ? o : 0u; v[3] = (d >> 1) == 3 ? o : 0u;
            lds_st<u32x4>(sm + K2_LUTS + row * 80 + sh * 16, v);
        } else if (tid >= 128 && tid < 256) lds_st<u32>(sm + K2_HIST + (tid - 128) * 4, 0u);
        else if (tid >= 256 && tid < 272) lds_st<u32>(sm + K2_POL + (tid - 256) * 4, 0u);
    }
    asm volatile("s_waitcnt vmcnt(7)" : "+v"(oc) :: "memory");
    ST(1, 1);
    const u32 cnt = (tid >> 2) < NCH ? (oc >> 16) : 0u;
    const u32* rp = (const u32*)(tab + REC_OFF) + cell * CH + (oc & 0xffffu) + q4 * 4;
    const bool two = __any(cnt > 16u);
    u32x4 ra, rb = {0u, 0u, 0u, 0u}, aux = {0u, 0u, 0u, 0u};
    asm volatile("global_load_dwordx4 %0, %1, off" : "=v"(ra) : "v"(rp) : "memory");
    {
        const char* ap = tab + POL_OFF + min(w, 2) * 1024 + lane * 16;
        asm volatile("global_load_dwordx4 %0, %1, off" : "=v"(aux) : "v"(ap) : "memory");
    }
    asm volatile("s_waitcnt vmcnt(2)" ::: "memory");
    wg_barrier();
    ST(1, 2);

    bf16x8 af0, af1, wcf; u32x4 up0, up1;
    {
        const int q = (lane >> 2) & 3, p = lane & 3, cb = (lane >> 4) & 1;
        const u32 wo = (8 * h + q) * 256 + (((4 * m + 2 * cb + (p >> 1)) ^ (q << 2)) << 4) + (p & 1) * 8;
        const u32 wad = sm + K2_W + wo;
        const u32 xb = sm + K2_X + (g * 8 + (r >> 2)) * XP + (r & 3) * 256;
        const u32 xa0 = xb + ((h ^ (r & 3)) << 4), xa1 = xb + (((2 + h) ^ (r & 3)) << 4);
        f32x16 aa = {}, au = {};
#pragma unroll
        for (int s = 0; s < 8; ++s) {
            bf16x8 xf = lds_ld<bf16x8>(((s & 1) ? xa1 : xa0) + (s >> 1) * 64);
            bf16x8 fa = lds_tr2(wad + s * 4096, wad + s * 4096 + 1024);
            bf16x8 fu = lds_tr2(wad + 32768 + s * 4096, wad + 32768 + s * 4096 + 1024);
            aa = MFMA(xf, fa, aa); au = MFMA(xf, fu, au);
        }
        u32x4 a0, a1, c4 = __builtin_bit_cast(u32x4, lds_tr2(sm + K2_WC + wo, sm + K2_WC + wo + 1024));
#pragma unroll
        for (int i = 0; i < 4; ++i) {
            a0[i] = pk_bf16(aa[2 * i] * NLOG2E, aa[2 * i + 1] * NLOG2E);
            a1[i] = pk_bf16(aa[8 + 2 * i] * NLOG2E, aa[9 + 2 * i] * NLOG2E);
            up0[i] = pk_bf16(au[2 * i], au[2 * i + 1]);
            up1[i] = pk_bf16(au[8 + 2 * i], au[9 + 2 * i]);
            c4[i] = pk_bf16(bf_lo(c4[i]) * NLOG2E, bf_hi(c4[i]) * NLOG2E);
        }
        af0 = __builtin_bit_cast(bf16x8, a0); af1 = __builtin_bit_cast(bf16x8, a1); wcf = __builtin_bit_cast(bf16x8, c4);
    }
    ST(1, 3);
    asm volatile("s_waitcnt vmcnt(0)" : "+v"(ra), "+v"(aux) :: "memory");
    if (two) rb = *(const u32x4u*)(rp + 16);
    if (w == 0) {
        const u32 me = xcc_id();
#pragma unroll
        for (int k = 0; k < 4; ++k) if ((aux[k] & 15u) == me) lds_add(sm + K2_POL + (((lane * 4 + k) >> 3) % 13) * 4, (u32)((int)aux[k] >> 4));
    } else if (w < 3) {
        u32 f = aux[0] | aux[1] | aux[2] | aux[3]; f |= f >> 16; f |= f >> 8;
        lds_or(sm + K2_POL + 60, f & 7u);
    }
    if (cnt > 32u) lds_or(sm + K2_POL + 60, 1u);
    u32 rka[4], rkb[4];
    auto key_of = [](u32 v) { const u32 qd = v / CH, s = qd / NB, dl = qd - s * NB; return (dl & 3u) * 32u + ((s * 83u) >> 16); };
#pragma unroll
    for (int k = 0; k < 4; ++k) { rka[k] = 0; rkb[k] = 0; if ((u32)(q4 * 4 + k) < cnt) rka[k] = lds_add(sm + K2_HIST + key_of(ra[k]) * 4, 1u); }
    if (two) {
#pragma unroll
        for (int k = 0; k < 4; ++k) if ((u32)(16 + q4 * 4 + k) < cnt) rkb[k] = lds_add(sm + K2_HIST + key_of(rb[k]) * 4, 1u);
    }
    wg_barrier();
    ST(1, 4);
    int T, Tmax; u32 polmask;
    {
        const u32x2 c = lds_ld<u32x2>(sm + K2_HIST + lane * 8);
        const int ps = lds_ld<int>(sm + K2_POL + (lane & 15) * 4);
        const u32 inc = row_scan(c[0] + c[1]);
        const u32 b0 = (lane >> 4) * 512 + inc - c[0] - c[1];
        lds_st<u32x2>(sm + K2_BASE + lane * 8, u32x2{b0, b0 + c[0]});
        const u32 n0 = __builtin_amdgcn_readlane(inc, 15), n1 = __builtin_amdgcn_readlane(inc, 31), n2 = __builtin_amdgcn_readlane(inc, 47), n3 = __builtin_amdgcn_readlane(inc, 63);
        const u32 nmax = max(max(n0, n1), max(n2, n3)), fl = (u32)__builtin_amdgcn_readlane(ps, 15);
        if (nmax > 512u || (fl & 1u) || (fl & 6u) == 4u) { slow_bucket(x, ea, wa, wb, wc, wu, wv, tab, out, bucket, sm); return; }
        T = (int)(((g == 0 ? n0 : g == 1 ? n1 : g == 2 ? n2 : n3) + 31u) >> 5); Tmax = (int)((nmax + 31u) >> 5);
        polmask = (u32)__ballot(ps > 0) & 0x1fffu;
        lds_st<u32x4>(sm + K2_X + w * 2048 + lane * 16, up0);
        lds_st<u32x4>(sm + K2_X + w * 2048 + 1024 + lane * 16, up1);
        asm volatile("s_waitcnt lgkmcnt(0)" ::: "memory");
        auto place = [&](u32 v, u32 rk) {
            const u32 qd = v / CH, el = v - qd * CH, s = qd / NB, dl = qd - s * NB;
            const u32 pos = lds_ld<volatile u32>(sm + K2_BASE + ((dl & 3u) * 32u + ((s * 83u) >> 16)) * 4) + rk;
            const u32 tb = pos & ~31u, rho = pos & 31u, hh = (rho >> 2) & 1u, ln = dl >> 2;
            const u32 to = (s >> 4) * 16384u + ((polmask >> (s >> 11)) & 1u) * 8192u + (s & 15u) * 512u + (hh ? 0u : 128u);
            lds_st<u32>(sm + K2_TOFF + (tb + hh * 16u + (rho & 3u) + ((rho >> 3) << 2)) * 4, to);
            lds_st<u32>(sm + K2_EOFF + pos * 4, (cell * CH + el) * 32u);
            lds_st<u16>(sm + K2_DLT + (tb + ((rho >> 3) & 1u) * 16u + (rho & 3u) * 4u + hh + ((rho >> 4) << 1)) * 2, (u16)(ln * 256u));
            lds_st<u16>(sm + K2_DLS + pos * 2, (u16)(ln * 80u));
        };
#pragma unroll
        for (int k = 0; k < 4; ++k) if ((u32)(q4 * 4 + k) < cnt) place(ra[k], rka[k]);
        if (two) {
#pragma unroll
            for (int k = 0; k < 4; ++k) if ((u32)(16 + q4 * 4 + k) < cnt) place(rb[k], rkb[k]);
        }
    }
    wg_barrier();
    ST(1, 5);

    const auto rt = __builtin_amdgcn_make_buffer_rsrc((void*)(tab + m * 128 - 128), 4, TAB_BYTES + 128, 0x00800000);
    const auto re = __builtin_amdgcn_make_buffer_rsrc((void*)ea, 0, NE * 32, 0x00020000);
    const u32 a_toff = sm + K2_TOFF + g * 2048 + h * 64;
    const int er = 8 * m + (lane >> 3);
    const u32 a_eoff = sm + K2_EOFF + g * 2048 + er * 4;
    const u32 a_ew = sm + K2_W + g * 16384 + (2 * er + (((lane >> 2) & 1) ^ (m & 1))) * 16 + (lane & 3) * 4;
    const u32 a_er = sm + K2_W + g * 16384 + (2 * r + (h ^ ((r >> 3) & 1))) * 16;
    const u32 a_dls = sm + K2_DLS + g * 1024 + r * 2;
    const u32 a_dlt = sm + K2_DLT + g * 1024 + h * 32 + ((lane >> 2) & 3) * 8;
    const u32 c_lutt = sm + K2_LUTT + ((lane >> 2) & 3) * 64 + ((lane >> 4) & 1) * 32 + (lane & 3) * 8;
    const u32 c_luts = sm + K2_LUTS + h * 16;
    const u32 elane = (lane & 7) * 4;
    f32x16 num = {}, den = {}, dA, dB;
    u32 eA, eB;
    auto load_rows = [&](f32x16& d, int t) {
#pragma unroll
        for (int a = 0; a < 4; ++a) {
            const u32x4 o = lds_ld<u32x4>(a_toff + t * 128 + a * 16);
#pragma unroll
            for (int i = 0; i < 4; ++i) d[4 * a + i] = __uint_as_float(__builtin_amdgcn_raw_buffer_load_b32(rt, o[i], 0, 0));
        }
    };
    auto load_e = [&](int t) { return __builtin_amdgcn_raw_buffer_load_b32(re, lds_ld<u32>(a_eoff + min(t, 15) * 128) + elane, 0, 0); };
    {
        const u32 e0 = load_e(0);
        load_rows(dA, 0);
        eA = load_e(1); eB = load_e(2);
        lds_st<u32>(a_ew, e0);
        wg_barrier();
    }
    ST(1, 6);
    auto step = [&](f32x16& cur, f32x16& nxt, u32& eo, int t) {
        const u32 dls = lds_ld<u16>(a_dls + t * 64);
        const u32x2 dlt = lds_ld<u32x2>(a_dlt + t * 64);
        const bf16x8 ef = lds_ld<bf16x8>(a_er + t * 1024);
        load_rows(nxt, min(t + 1, 15));
        if (t + 1 < T) lds_st<u32>(a_ew + (t + 1) * 1024, eo);
        eo = load_e(t + 3);
        const bf16x8 s0 = lds_ld<bf16x8>(c_luts + dls), s1 = lds_ld<bf16x8>(c_luts + dls + 32);
        f32x16 tt = MFMA(ef, wcf, cur);
        tt = MFMA(s0, af0, tt);
        tt = MFMA(s1, af1, tt);
        const bf16x8 st0 = lds_tr2(c_lutt + (dlt[0] & 0xffffu), c_lutt + (dlt[0] >> 16));
        const bf16x8 st1 = lds_tr2(c_lutt + (dlt[1] & 0xffffu), c_lutt + (dlt[1] >> 16));
#pragma unroll
        for (int s = 0; s < 2; ++s) {
            f32x8 eta, pv;
#pragma unroll
            for (int i = 0; i < 8; ++i) {
                eta[i] = __builtin_amdgcn_rcpf(1.f + __builtin_amdgcn_exp2f(tt[8 * s + i]));
                const _Float16 v = __builtin_bit_cast(_Float16, (u16)__float_as_uint(cur[8 * s + i]));
                pv[i] = __builtin_fmaf(eta[i], (float)v, -0.f);
            }
            den = __builtin_amdgcn_smfmac_f32_32x32x32_bf16(s ? st1 : st0, __builtin_bit_cast(bf16x16, eta), den, 0xdddd, 0, 0);
            num = __builtin_amdgcn_smfmac_f32_32x32x32_bf16(s ? st1 : st0, __builtin_bit_cast(bf16x16, pv), num, 0xdddd, 0, 0);
            if (s == 0) { __builtin_amdgcn_sched_barrier(0); wg_barrier(); __builtin_amdgcn_sched_barrier(0); }
        }
    };
    int t = 0;
    for (; t + 1 < T; t += 2) { step(dA, dB, eA, t); step(dB, dA, eB, t + 1); }
    if (t < T) { step(dA, dB, eA, t); ++t; }
    STW(1, 7, 0); STW(1, 8, 15);
    {
        const u32x4 u0 = lds_ld<u32x4>(sm + K2_X + w * 2048 + lane * 16), u1 = lds_ld<u32x4>(sm + K2_X + w * 2048 + 1024 + lane * 16);
        const u32 sa = sm + K2_W + g * 16384 + h * 4 * 272 + m * 64 + r * 2;
#pragma unroll
        for (int i = 0; i < 14; i += 2) {
            const u32 uu = i < 8 ? u0[i >> 1] : u1[(i - 8) >> 1];
            const float z0 = bf_lo(uu) + num[i] * __builtin_amdgcn_rcpf(den[i] + 1e-6f);
            const float z1 = bf_hi(uu) + num[i + 1] * __builtin_amdgcn_rcpf(den[i + 1] + 1e-6f);
            const u32 pz = pk_bf16(fmaxf(z0, 0.f), fmaxf(z1, 0.f));
            const int row = (i & 3) + 8 * (i >> 2);
            lds_st<u16>(sa + row * 272, (u16)pz);
            lds_st<u16>(sa + (row + 1) * 272, (u16)(pz >> 16));
        }
    }
    const int c0 = tid, c1 = tid + 1024;
    const bool v0 = node0 + (c0 >> 4) < NN, v1 = c1 < NB * 16 && node0 + (c1 >> 4) < NN;
    u32x4 x0 = {0u, 0u, 0u, 0u}, x1 = {0u, 0u, 0u, 0u};
    if (v0) x0 = *(const u32x4*)(x + node0 * 128 + c0 * 8);
    if (v1) x1 = *(const u32x4*)(x + node0 * 128 + c1 * 8);
    for (; t <= Tmax; ++t) wg_barrier();
    auto fin = [&](int c, u32x4 xv) {
        const int dl = c >> 4;
        const u32x4 z = lds_ld<u32x4>(sm + K2_W + (dl & 3) * 16384 + (dl >> 2) * 272 + (c & 15) * 16);
        u32x4 o;
#pragma unroll
        for (int i = 0; i < 4; ++i) o[i] = pk_bf16(bf_lo(xv[i]) + bf_lo(z[i]), bf_hi(xv[i]) + bf_hi(z[i]));
        *(u32x4*)(out + node0 * 128 + c * 8) = o;
    };
    if (v0) fin(c0, x0);
    if (v1) fin(c1, x1);
    ST(1, 9);
}

extern "C" void kernel_launch(void* const* d_in, const int* in_sizes, int n_in, void* d_out, int out_size, void* d_ws, size_t ws_size,
                              hipStream_t stream) {
    const u16* x = (const u16*)d_in[0];
    const u16* ea = (const u16*)d_in[1];
    const int* src = (const int*)d_in[2];
    const int* dst = (const int*)d_in[3];
    const u16* wa = (const u16*)d_in[4];
    const u16* wb = (const u16*)d_in[5];
    const u16* wc = (const u16*)d_in[6];
    const u16* wu = (const u16*)d_in[7];
    const u16* wv = (const u16*)d_in[8];
    k1<<<256, 1024, 0, stream>>>(x, src, dst, wb, wv, (char*)d_ws);
    k2<<<256, 1024, 0, stream>>>(x, ea, wa, wb, wc, wu, wv, (char*)d_ws, (u16*)d_out);
}
```

```cpp
#define PHI 2
#include <hip/hip_runtime.h>
#include <stdint.h>

typedef unsigned u32;
typedef unsigned long long u64;
typedef unsigned short u16;
typedef float f32x16 __attribute__((ext_vector_type(16)));
typedef float f32x4 __attribute__((ext_vector_type(4)));
typedef float f32x2 __attribute__((ext_vector_type(2)));
typedef __bf16 bf16x2 __attribute__((ext_vector_type(2)));
typedef _Float16 f16x2 __attribute__((ext_vector_type(2)));
typedef __bf16 bf16x8 __attribute__((ext_vector_type(8)));
typedef short s16x4 __attribute__((ext_vector_type(4)));
typedef u32 u32x2 __attribute__((ext_vector_type(2)));
typedef u32 u32x4 __attribute__((ext_vector_type(4)));
typedef u32 u32x8 __attribute__((ext_vector_type(8)));
#define LAS __attribute__((address_space(3)))
#define GAS __attribute__((address_space(1)))
#define DI __device__ __forceinline__

constexpr int NN = 25000, NE = 400000, NB = 98, CH = 1600, NCH = 250;
constexpr int XP = 1088;
constexpr u32 TAB_BYTES = 13u << 21;
constexpr u32 REC_OFF = TAB_BYTES;
constexpr u32 OFF_OFF = REC_OFF + 3202048;
constexpr u32 POL_OFF = OFF_OFF + 524288;
constexpr u32 DON_OFF = POL_OFF + 2048;
constexpr u32 EPO_OFF = DON_OFF + 32768;
constexpr u32 SPIN_CAP = 1u << 18;
constexpr u32 LP = 144;
constexpr float LOG2E = 1.4426950408889634f;
#define PF_UNITS 8
#define PF_BINS 5
#define FIRST_G 2
#define TAIL_PRIO 0
#define ORD 0
#define POLL_SLEEP 8
#define W1_G 1
#define WFIRST 2
#define X1_AT 3
#define A_GATE 0
#define PRE_CODE 4
#define PRE_TLB 0
#define OUT_POL " sc1"
#define D_PRIO 0
#define C_CUT 8
#define C_CUT2 16
#define D_DELAY 12
#define IDX_AT 0
#define W2_EARLY 0
#define LATE_POST 0
#define LATE_W 8
#define EMASK 0x00ffffe0u
#define TMASK 0xffffu

#define ST_PAD 0
#define ST_DECL
#define ST(i)
#define ST_OUT(k)

DI char* tab_base(char* ws) { return ws + ((0u - (u32)(u64)ws) & 0x1fffffu); }
template <class T> DI T lds_ld(u32 a) { return *(const LAS T*)a; }
template <class T> DI void lds_st(u32 a, T v) { *(LAS T*)a = v; }
DI u32 lds_add(u32 a, u32 v) { return __hip_atomic_fetch_add((LAS u32*)a, v, __ATOMIC_RELAXED, __HIP_MEMORY_SCOPE_WORKGROUP); }
DI void lds_or(u32 a, u32 v) { (void)__hip_atomic_fetch_or((LAS u32*)a, v, __ATOMIC_RELAXED, __HIP_MEMORY_SCOPE_WORKGROUP); }
DI s16x4 lds_tr(u32 a) { return __builtin_amdgcn_ds_read_tr16_b64_v4i16((LAS s16x4*)a); }
DI bf16x8 lds_tr2(u32 a0, u32 a1) {
    s16x4 lo = lds_tr(a0), hi = lds_tr(a1);
    return __builtin_bit_cast(bf16x8, __builtin_shufflevector(lo, hi, 0, 1, 2, 3, 4, 5, 6, 7));
}
DI void glds16(const void* g, u32 dst) {
    u32 keep;
    asm volatile("s_mov_b32 %0, m0\n\ts_mov_b32 m0, %2\n\ts_nop 0\n\tglobal_load_lds_dwordx4 %1, off\n\ts_mov_b32 m0, %0"
                 : "=&s"(keep) : "v"(g), "s"(dst) : "memory");
}
#define X_NT ""
#define E_NT ""
#define E_AUX0 0
DI void glds16x(const void* g, u32 dst) {
    u32 keep;
    asm volatile("s_mov_b32 %0, m0\n\ts_mov_b32 m0, %2\n\ts_nop 0\n\tglobal_load_lds_dwordx4 %1, off" X_NT "\n\ts_mov_b32 m0, %0"
                 : "=&s"(keep) : "v"(g), "s"(dst) : "memory");
}
DI void glds16x4(const void* g, u32 dst) {
    u32 keep;
    asm volatile("s_mov_b32 %0, m0\n\ts_mov_b32 m0, %2\n\ts_nop 0\n\tglobal_load_lds_dwordx4 %1, off\n\tglobal_load_lds_dwordx4 %1, off offset:1024\n\t"
                 "global_load_lds_dwordx4 %1, off offset:2048\n\tglobal_load_lds_dwordx4 %1, off offset:3072\n\ts_mov_b32 m0, %0"
                 : "=&s"(keep) : "v"(g), "s"(dst) : "memory");
}
DI void wg_barrier() { asm volatile("s_waitcnt lgkmcnt(0)\n\ts_barrier" ::: "memory"); }
DI u32 pk_bf16(float a, float b) { f32x2 v = {a, b}; return __builtin_bit_cast(u32, __builtin_convertvector(v, bf16x2)); }
DI u32 pk_f16(float a, float b) { f32x2 v = {a, b}; return __builtin_bit_cast(u32, __builtin_convertvector(v, f16x2)); }
DI float bf_lo(u32 v) { return __uint_as_float(v << 16); }
DI float bf_hi(u32 v) { return __uint_as_float(v & 0xffff0000u); }
DI float bf2f(u16 v) { return __uint_as_float((u32)v << 16); }
DI float solo(float v) { asm("" : "+v"(v)); return v; }
#define MFMA(a, b, c) __builtin_amdgcn_mfma_f32_32x32x16_bf16((a), (b), (c), 0, 0, 0)
template <int CTRL> DI u32 dpp0(u32 v) { return __builtin_amdgcn_update_dpp(0u, v, CTRL, 0xf, 0xf, true); }
DI u32 row_scan(u32 v) { v += dpp0<0x111>(v); v += dpp0<0x112>(v); v += dpp0<0x114>(v); v += dpp0<0x118>(v); return v; }
template <int CTRL, int RM> DI u32 dpp_max(u32 v) { return max(v, (u32)__builtin_amdgcn_update_dpp(0u, v, CTRL, RM, 0xf, true)); }
DI u32 wave_max_scan(u32 v) {
    v = dpp_max<0x111, 0xf>(v); v = dpp_max<0x112, 0xf>(v); v = dpp_max<0x114, 0xf>(v); v = dpp_max<0x118, 0xf>(v);
    v = dpp_max<0x142, 0xa>(v); v = dpp_max<0x143, 0xc>(v); return v;
}
DI int fresh_lane() { int l; asm volatile("v_mbcnt_lo_u32_b32 %0, -1, 0\n\tv_mbcnt_hi_u32_b32 %0, -1, %0" : "=v"(l)); return l; }
DI u32 xcc_id() { return (u32)__builtin_amdgcn_s_getreg((3 << 11) | 20) & 0xfu; }
DI bool once_unit(u32 unit) { return PHI && (unit & (PHI - 1)) == PHI - 1; }
DI u32 side_of(u32 unit, u32 polmask) { return once_unit(unit) ? (unit / (PHI ? PHI : 1)) & 1u : (polmask >> (unit >> 7)) & 1u; }
DI void lds_wait(u32 a, u32 n) { while (lds_ld<volatile u32>(a) < n) __builtin_amdgcn_s_sleep(1); asm volatile("" ::: "memory"); }
DI void lds_one(u32 a, u32 v, bool is_or) {
    if (is_or) asm volatile("s_mov_b64 exec, 1\n\tds_or_b32 %0, %1\n\ts_mov_b64 exec, -1" :: "v"(a), "v"(v) : "memory");
    else asm volatile("s_waitcnt lgkmcnt(0)\n\ts_mov_b64 exec, 1\n\tds_add_u32 %0, %1\n\ts_mov_b64 exec, -1" :: "v"(a), "v"(v) : "memory");
}
DI void lds_post(u32 a, int) { lds_one(a, 1u, false); }
DI u32 opaque(u32 a) { asm("" : "+v"(a)); return a; }
DI u32 w_addr(int ln, int m) { const int q = (ln >> 2) & 3, p = ln & 3; return (8 * (ln >> 5) + q) * 256 + (((4 * m + 2 * ((ln >> 4) & 1) + (p >> 1)) ^ (q << 2)) << 4) + (p & 1) * 8; }

DI u32x2 ld_word(const char* p) { return __builtin_bit_cast(u32x2, __hip_atomic_load((const GAS u64*)p, __ATOMIC_RELAXED, __HIP_MEMORY_SCOPE_AGENT)); }
DI void st_word(char* p, u32 v, u32 tag) { __hip_atomic_store((GAS u64*)p, ((u64)tag << 32) | v, __ATOMIC_RELAXED, __HIP_MEMORY_SCOPE_AGENT); }

constexpr u32 L_HIST = 0;
constexpr u32 L_BASE = L_HIST + 512;
constexpr u32 L_CNT = L_BASE + 512;
constexpr u32 C_HIST = L_CNT, C_DMA = L_CNT + 4, C_REC = L_CNT + 8, C_OUT = L_CNT + 12, C_FRAG = L_CNT + 16, C_TRA = L_CNT + 20, C_TR = L_CNT + 24,
              C_DRAIN = L_CNT + 28, C_VFL = L_CNT + 32, C_FLAG = L_CNT + 36, C_ACK = L_CNT + 40, C_GO = L_CNT + 44, L_NW = L_CNT + 48, L_POL = L_CNT + 64, C_DRAIN1 = L_CNT + 68, C_FREE = L_CNT + 72, C_DMA1 = L_CNT + 76, C_W1 = L_CNT + 80;
constexpr u32 L_LUT = L_CNT + 256;
constexpr u32 L_WC = L_LUT + 4864;
constexpr u32 L_W1 = L_WC + 4096;
constexpr u32 L_LT = L_W1;
constexpr u32 L_LR = L_LT + 8192;
constexpr u32 L_E = L_LR + 16384;
constexpr u32 L_REC = L_W1 + 32768, L_HIST1 = L_REC + CH * 8, L_BASE1 = L_HIST1 + 1024;
constexpr u32 L_MK = L_REC;
constexpr u32 L_X = L_W1 + 49152;
constexpr u32 L_W = L_X + 33 * XP;
constexpr u32 L_END = L_W + 65536;
static_assert(L_END <= 163840 && L_E % 256 == 0 && L_BASE1 + 512 <= L_X && L_W % 16 == 0, "");

DI void slow_bucket(const u16* x, const u16* ea, const int* src, const int* dst, const u16* wa, const u16* wb, const u16* wc, const u16* wu,
                    const u16* wv, const char* tab, u16* out, int bucket, u32 sm, int w) {
    const int tid = w * 64 + fresh_lane(), c = tid & 127, sub = tid >> 7, node0 = bucket * NB;
    __syncthreads();
    for (int i = tid; i < NB * 256; i += 1024) lds_st<float>(sm + i * 4, 0.f);
    __syncthreads();
    for (int cell = 0; cell < NCH; ++cell) {
        const u32 oc = ld_word(tab + OFF_OFF + (cell * 256 + bucket) * 8)[0];
        for (u32 k = (u32)(sub + cell) & 7u; k < (oc >> 16); k += 8) {
            const u32 e = (ld_word(tab + REC_OFF + (cell * CH + (oc & 0xffffu) + k) * 8)[1] & 0xffffffu) >> 5;
            const int s = src[e], dl = dst[e] - node0;
            float g = 0.f, hv = 0.f;
#pragma unroll 4
            for (int kk = 0; kk < 128; ++kk) {
                const float xs = bf2f(x[s * 128 + kk]);
                g += xs * bf2f(wb[kk * 128 + c]) + bf2f(x[(node0 + dl) * 128 + kk]) * bf2f(wa[kk * 128 + c]);
                hv += xs * bf2f(wv[kk * 128 + c]);
            }
#pragma unroll 4
            for (int kk = 0; kk < 16; ++kk) g += bf2f(ea[e * 16 + kk]) * bf2f(wc[kk * 128 + c]);
            const float eta = 1.f / (1.f + __expf(-g));
            __hip_atomic_fetch_add((LAS float*)(sm + (dl * 256 + c) * 4), eta * hv, __ATOMIC_RELAXED, __HIP_MEMORY_SCOPE_WORKGROUP);
            __hip_atomic_fetch_add((LAS float*)(sm + (dl * 256 + 128 + c) * 4), eta, __ATOMIC_RELAXED, __HIP_MEMORY_SCOPE_WORKGROUP);
        }
    }
    __syncthreads();
    for (int dl = sub; dl < NB && node0 + dl < NN; dl += 8) {
        float u = 0.f;
#pragma unroll 4
        for (int kk = 0; kk < 128; ++kk) u += bf2f(x[(node0 + dl) * 128 + kk]) * bf2f(wu[kk * 128 + c]);
        const float z = u + lds_ld<float>(sm + (dl * 256 + c) * 4) / (lds_ld<float>(sm + (dl * 256 + 128 + c) * 4) + 1e-6f);
        out[(node0 + dl) * 128 + c] = (u16)pk_bf16(bf2f(x[(node0 + dl) * 128 + c]) + fmaxf(z, 0.f), 0.f);
    }
}

#define S_(x) #x
#define XS(x) S_(x)
#define G8(op, d, o) op(d, o, 0) op(d, o, 1) op(d, o, 2) op(d, o, 3) op(d, o, 4) op(d, o, 5) op(d, o, 6) op(d, o, 7)
#define I_EXP(d, o, i) "v_exp_f32 v[64+" #o "+" #i "], v[64+" #o "+" #i "]\n\t"
#define I_RCP(d, o, i) "v_rcp_f32 v[64+" #o "+" #i "], v[64+" #o "+" #i "]\n\t"
#define I_MIX(d, o, i) "v_fma_mix_f32 v[" #d "+" #o "+" #i "], v[64+" #o "+" #i "], v[" #d "+" #o "+" #i "], 0 op_sel_hi:[0,1,0]\n\t"
#define I_LD(d, o, i) "buffer_load_dword v[" #d "+" #o "+" #i "], v[" #d "+" #o "+" #i "], %[rt], 0 offen sc1\n\t"
#define HALF(cur, o, st)                                                                                                   \
    G8(I_RCP, cur, o) G8(I_MIX, cur, o)                                                                                    \
    "v_smfmac_f32_32x32x32_bf16 v[16:31], v[" #st ":" #st "+3], v[64+" #o ":64+" #o "+7], v123\n\t"                        \
    "s_nop 0\n\t"                                                                                                          \
    "v_smfmac_f32_32x32x32_bf16 v[0:15], v[" #st ":" #st "+3], v[" #cur "+" #o ":" #cur "+" #o "+7], v123\n\t"
#define SDWA_HI " dst_sel:DWORD dst_unused:UNUSED_PAD src0_sel:WORD_1 src1_sel:DWORD\n\t"
#define STEP(cur, nxt, b, L, VM, PRE, MID)                                                                                 \
    "ds_read_b128 v[92:95], v118 offset:" #b "*1024\n\t"                                                                   \
    "ds_read_b32 v113, v115 offset:" #b "*256\n\t"                                                                         \
    "ds_read2_b32 v[100:101], v116 offset0:" #b "*64 offset1:" #b "*64+16\n\t"                                             \
    "ds_read2_b32 v[102:103], v116 offset0:" #b "*64+32 offset1:" #b "*64+48\n\t"                                          \
    "ds_read_b128 v[" #nxt ":" #nxt "+3], v114 offset:" #b "*128+128\n\t"                                                  \
    "ds_read_b128 v[" #nxt "+4:" #nxt "+7], v114 offset:" #b "*128+144\n\t"                                                \
    "ds_read_b128 v[" #nxt "+8:" #nxt "+11], v114 offset:" #b "*128+192\n\t"                                               \
    "ds_read_b128 v[" #nxt "+12:" #nxt "+15], v114 offset:" #b "*128+208\n\t"                                              \
    "s_sub_i32 %[hl], %[hl], 2\n\t"                                                                                        \
    "s_waitcnt vmcnt(" #VM ") lgkmcnt(4)\n\t"                                                                              \
    "v_mfma_f32_32x32x16_bf16 v[64:79], v[92:95], v[88:91], v[" #cur ":" #cur "+15]\n\t"                                   \
    "v_add_u32_sdwa v113, v113, v120" SDWA_HI                                                                              \
    "v_add_u32_sdwa v100, v100, v121" SDWA_HI                                                                              \
    "v_add_u32_sdwa v101, v101, v121" SDWA_HI                                                                              \
    "v_add_u32_sdwa v102, v102, v121" SDWA_HI                                                                              \
    "v_add_u32_sdwa v103, v103, v121" SDWA_HI                                                                              \
    "ds_read_b128 v[96:99], v113 offset:64\n\t"                                                                            \
    "ds_read_b32 v112, v113 offset:96\n\t"                                                                                 \
    "ds_read_b64_tr_b16 v[104:105], v100\n\t"                                                                              \
    "ds_read_b64_tr_b16 v[106:107], v101\n\t"                                                                              \
    "ds_read_b64_tr_b16 v[108:109], v102\n\t"                                                                              \
    "ds_read_b64_tr_b16 v[110:111], v103\n\t"                                                                              \
    PRE                                                                                                                    \
    "s_cmp_gt_i32 %[hl], 0\n\t"                                                                                            \
    "s_cbranch_scc0 " L "1%=\n\t"                                                                                          \
    "s_waitcnt lgkmcnt(6)\n\t"                                                                                             \
    G8(I_LD, nxt, 0)                                                                                                       \
    G8(I_EXP, cur, 0) G8(I_EXP, cur, 8)                                                                                    \
    "s_nop 0\n\ts_waitcnt lgkmcnt(4)\n\t"                                                                                  \
    "v_smfmac_f32_32x32x32_bf16 v[64:79], v[96:99], v[80:87], v112\n\t"                                                    \
    G8(I_LD, nxt, 8)                                                                                                       \
    "s_branch " L "2%=\n"                                                                                                  \
    L "1%=:\n\t"                                                                                                           \
    "s_cmp_lt_i32 %[hl], 0\n\t"                                                                                            \
    G8(I_EXP, cur, 0)                                                                                                      \
    "s_cbranch_scc1 " L "3%=\n\t"                                                                                          \
    G8(I_EXP, cur, 8)                                                                                                      \
    L "3%=:\n\t"                                                                                                           \
    "s_nop 0\n\ts_waitcnt lgkmcnt(4)\n\t"                                                                                  \
    "v_smfmac_f32_32x32x32_bf16 v[64:79], v[96:99], v[80:87], v112\n\t"                                                    \
    "s_nop 8\n"                                                                                                            \
    L "2%=:\n\t"                                                                                                           \
    MID                                                                                                                    \
    HALF(cur, 0, 104)                                                                                                      \
    "s_cmp_lt_i32 %[hl], 0\n\t"                                                                                            \
    "s_cbranch_scc1 LEND%=\n\t"                                                                                            \
    HALF(cur, 8, 108)                                                                                                      \
    "s_cmp_eq_i32 %[hl], 0\n\t"                                                                                            \
    "s_cbranch_scc1 LEND%=\n\t"
#define MID_EVEN "s_waitcnt lgkmcnt(0)\n\ts_setprio 1\n\ts_nop 0\n\t"
#define MID_ODD(b, L, CHECK)                                                                                               \
    "s_sub_i32 %[nb], %[nb], 1\n\ts_cmp_lt_i32 %[nb], 0\n\ts_cbranch_scc1 " L "4%=\n\t"       \
    "ds_write2st64_b32 v119, v124, v125 offset0:((" #b "+1)&3)*4 offset1:((" #b "+2)&3)*4\n\t"                             \
    "ds_read2st64_b32 v[100:101], v117 offset0:" #b "+3 offset1:" #b "+4\n\t"                                              \
    "s_waitcnt lgkmcnt(0)\n\t"                                                                                             \
    "v_and_or_b32 v124, v100, %[em], v122\n\tv_and_or_b32 v125, v101, %[em], v122\n\t"                                     \
    CHECK                                                                                                                  \
    "s_barrier\n\t"                                                                                                        \
    "buffer_load_dword v124, v124, %[re], 0 offen\n\tbuffer_load_dword v125, v125, %[re], 0 offen\n"                       \
    L "4%=:\n\ts_waitcnt lgkmcnt(0)\n\ts_setprio 3\n\t"

__global__ __launch_bounds__(1024, 4) void gatedgcn(char* __restrict__ ws, const u16* __restrict__ x, const int* __restrict__ src, const int* __restrict__ dst,
                                                    const u16* __restrict__ wb, const u16* __restrict__ wv, const u16* __restrict__ wc, const u16* __restrict__ wa,
                                                    const u16* __restrict__ wu, const u16* __restrict__ ea, u16* __restrict__ out) {
    __shared__ __attribute__((aligned(256))) char smem[L_END + ST_PAD];
    const u32 sm = (u32)(size_t)smem;
    const int tid = threadIdx.x, lane = tid & 63, w = __builtin_amdgcn_readfirstlane(tid >> 6);
    const int j = blockIdx.x, cls = j & 7, li = j >> 3, bucket = cls * 32 + li, node0 = bucket * NB;
    const int g = w >> 2, m = w & 3, r = lane & 31, h = lane >> 5, q = lane >> 4, c16 = lane & 15;
    char* tab = tab_base(ws);
    ST_DECL; ST(0);
    const bool extra = li < 8;
    const u32 ngemm = extra ? 13u : 12u;
    const u32x4 z4 = {0u, 0u, 0u, 0u};
    u32 tag;
    asm volatile("global_load_dword %0, %1, off sc1" : "=v"(tag) : "v"(tab + EPO_OFF) : "memory");

    if (w < 8) {
        const u16* xp = x + ((c16 ^ q) << 3);
        auto fetch_w = [&](int p) {
            const int pw = (p + 2 * li) & 63;
            glds16((pw < 32 ? wb : wv) + ((pw & 31) * 4 + q) * 128 + ((c16 ^ (q << 2)) << 3), sm + L_W + pw * 1024);
        };
        if (w < 2) fetch_w(w);
        glds16(xp + min(node0 + w * 4 + q, NN - 1) * 128, sm + L_X + w * XP);
        if (IDX_AT >= 1 && X1_AT == 0) glds16(xp + min(node0 + 32 + w * 4 + q, NN - 1) * 128, sm + L_X + (8 + w) * XP);
        if (IDX_AT <= 1) wg_barrier();
        if (tid < 264) {
            const int n = tid >> 3, ck = tid & 7;
            const int d = ck < 4 ? (n & 7) : ((n >> 2) & 1) * 4 + ((n >> 3) & 1) * 2 + (n & 1);
            const u32 o = (ck < 4 ? (n >> 3) == ck : ((n >> 4) & 1) == ck - 4) ? ((d & 1) ? 0x3f800000u : 0x3f80u) : 0u;
            u32x4 t; t[0] = (d >> 1) == 0 ? o : 0u; t[1] = (d >> 1) == 1 ? o : 0u; t[2] = (d >> 1) == 2 ? o : 0u; t[3] = (d >> 1) == 3 ? o : 0u;
            if (ck >= 6) t[0] = (n & 2) ? 0xeeeeu : 0x4444u;
            lds_st<u32x4>(sm + L_LUT + n * LP + ck * 16, t);
        }
        if (w >= 2) fetch_w(w);
#pragma unroll
        for (int p = 1; p < 8; ++p) {
            fetch_w(w + 8 * p);
            if (p == IDX_AT && IDX_AT > 1) wg_barrier();
            if (p == X1_AT) glds16(xp + min(node0 + 32 + w * 4 + q, NN - 1) * 128, sm + L_X + (8 + w) * XP);
        }
        if (w < 4) asm volatile("s_waitcnt vmcnt(0)" ::: "memory");
        else {
            glds16(xp + min(node0 + 64 + m * 8 + q, NN - 1) * 128, sm + L_X + (16 + 2 * m) * XP);
            glds16(xp + min(node0 + 68 + m * 8 + q, NN - 1) * 128, sm + L_X + (17 + 2 * m) * XP);
            glds16(wc + (m * 4 + q) * 128 + ((c16 ^ (q << 2)) << 3), sm + L_WC + m * 1024);
            asm volatile("s_waitcnt vmcnt(3)" ::: "memory");
        }

        lds_post(sm + C_DMA, lane);
    } else if (w < 15) {
        const int pt = tid - 512, ec = min(bucket, NCH - 1) * CH + min(pt, 399) * 4;
        auto fetch_w1 = [&]() {
            const u16* wp = (ORD ? wu : wa) + ((w - 13) * 64 + q) * 128 + ((c16 ^ (q << 2)) << 3);
#pragma unroll
            for (int p = 0; p < 4; ++p) glds16x4(wp + p * 2048, sm + L_W1 + (w - 13) * 16384 + p * 4096);
        };
        u32x4 dv, sv;
        __builtin_amdgcn_s_setprio(3);
        if (IDX_AT) wg_barrier();
        asm volatile("global_load_dwordx4 %0, %1, off" : "=v"(dv) : "v"(dst + ec) : "memory");
        asm volatile("global_load_dwordx4 %0, %1, off" : "=v"(sv) : "v"(src + ec) : "memory");
        if (!IDX_AT) wg_barrier();
        asm volatile("s_waitcnt vmcnt(0)" : "+v"(dv), "+v"(sv) :: "memory");

        if (bucket < NCH) {
            u32 b[4], rk[4] = {0u, 0u, 0u, 0u};
#pragma unroll
            for (int k = 0; k < 4; ++k) b[k] = __umul24(dv[k], 42800u) >> 22;
            if (pt < 400) {
#pragma unroll
                for (int k = 0; k < 4; ++k) rk[k] = lds_add(sm + L_HIST1 + b[k] * 4, 1u);
            }
            lds_post(sm + C_HIST, lane);
            u32x2 rv[4];
#pragma unroll
            for (int k = 0; k < 4; ++k) {
                const u32 bs = __umul24(sv[k], 42800u) >> 22, ds = sv[k] - __umul24(bs, NB);
                const u32 row = ds < 96u ? ((ds >> 5) << 13) + bs * 32u + (ds & 31u) : 24480u + 2u * bs + ds;
                const u32 dl = dv[k] - __umul24(b[k], NB), gg = __umul24(dl, 21u) >> 9, ln = dl - ((__umul24(gg, 49u) + 1u) >> 1);
                rv[k] = u32x2{(row + (row & ~15u)) | (__umul24(ln, LP) << 16), ((u32)(ec + k) << 5) | ((gg * 32u + (__umul24(row, 83u) >> 16)) << 24)};
            }
            asm volatile("" : "+v"(rv[0]), "+v"(rv[1]), "+v"(rv[2]), "+v"(rv[3]));
            lds_wait(sm + C_HIST, 7u);
            const u32x4 c = lds_ld<u32x4>(sm + L_HIST1 + lane * 16);
            const u32 tot = c[0] + c[1] + c[2] + c[3];
            u32 inc = row_scan(tot);
            const u32 r0 = __builtin_amdgcn_readlane(inc, 15), r1 = __builtin_amdgcn_readlane(inc, 31), r2 = __builtin_amdgcn_readlane(inc, 47);
            inc += (lane >= 16 ? r0 : 0) + (lane >= 32 ? r1 : 0) + (lane >= 48 ? r2 : 0);
            u32x4 st; st[0] = inc - tot; st[1] = st[0] + c[0]; st[2] = st[1] + c[1]; st[3] = st[2] + c[2];
            lds_st<u32x2>(sm + L_BASE1 + lane * 8, u32x2{st[0] | (st[1] << 16), st[2] | (st[3] << 16)});
            asm volatile("s_waitcnt lgkmcnt(0)" ::: "memory");
            if (pt < 400) {
#pragma unroll
                for (int k = 0; k < 4; ++k) rk[k] += lds_ld<u16>(sm + L_BASE1 + b[k] * 2);
#pragma unroll
                for (int k = 0; k < 4; ++k) lds_st<u32x2>(sm + L_REC + rk[k] * 8, rv[k]);
            }
            lds_post(sm + C_REC, lane);
            if (w >= 13) {
                lds_wait(sm + C_REC, 7u);
                const auto rr = __builtin_amdgcn_make_buffer_rsrc((void*)(tab + REC_OFF + bucket * (CH * 8)), 0, CH * 8, 0x00020000);
                const u32 o = (u32)(tid - 832) * 16u;
#pragma unroll
                for (int i = 0; i < 7; ++i) __builtin_amdgcn_raw_buffer_store_b128(lds_ld<u32x4>(sm + L_REC + i * 2048 + o), rr, i * 2048 + o, 0, 16);
                const u32 sb = lds_ld<u32>(sm + L_BASE1 + (o >> 2)), tg = __builtin_amdgcn_readfirstlane(tag) + 1u;
                const u32x2 cb = lds_ld<u32x2>(sm + L_HIST1 + (o >> 1));
                const u32x4 wd = {(sb & 0xffffu) | (cb[0] << 16), tg, (sb >> 16) | (cb[1] << 16), tg};
                asm volatile("" :: "v"(wd) : "memory");
                ST(5);
                fetch_w1();
                asm volatile("s_waitcnt vmcnt(16)" ::: "memory");
                lds_post(sm + C_ACK, lane);
                lds_wait(sm + C_ACK, 2u);
                const auto ro = __builtin_amdgcn_make_buffer_rsrc((void*)(tab + OFF_OFF + bucket * 2048), 0, 2048, 0x00020000);
                __builtin_amdgcn_raw_buffer_store_b128(wd, ro, o, 0, 16);
                ST(4);
            }
        }
        if (w >= 13) lds_post(sm + C_OUT, lane);
        if (w == 12) {
            const u16* xp = x + ((c16 ^ q) << 3);
            glds16(xp + min(node0 + 96 + q, NN - 1) * 128, sm + L_X + 24 * XP);
            if (extra) {
#pragma unroll 1
                for (int p = 0; p < 8; ++p) glds16(xp + min(((cls * 2 + (li >> 2)) * 16 + p * 2 + (q >> 1)) * NB + 96 + (q & 1), NN - 1) * 128, sm + L_X + (25 + p) * XP);
            }
            asm volatile("s_waitcnt vmcnt(0)" ::: "memory");
            lds_post(sm + C_W1, lane);
        }
        if (w >= 13) {
            if (bucket >= NCH) { fetch_w1(); asm volatile("s_waitcnt vmcnt(0)" ::: "memory"); } else asm volatile("s_waitcnt vmcnt(1)" ::: "memory");
            lds_post(sm + C_W1, lane);
            __builtin_amdgcn_s_setprio(D_PRIO);
        }
        ST(8);
    } else {
        lds_st<u32x4>(sm + L_HIST1 + lane * 16, z4);
        lds_st<u32x4>(sm + lane * 16, z4);
        if (lane < 16) lds_st<u32x4>(sm + 1024 + lane * 16, z4);
        const char* pa = tab + ((u32)min(li & 15, 12) << 21) + (u32)((li >> 2) * 8 + cls) * 16384u + (li & 3) * 256u;
        if (lane < 6) asm volatile("global_atomic_add %0, %1, off" :: "v"(pa + (lane & 1) * 8192 + (lane >> 1) * 1152), "v"(0u) : "memory");
        u32 t0_ = 0, t1_ = 0, t2_;
        {
            u64 pc; asm volatile("s_getpc_b64 %0" : "=s"(pc));
            t0_ = *(const GAS u32*)((pc & ~127ull) + (u32)(li * PRE_CODE + min(lane, PRE_CODE - 1)) * 128u);
        }
        wg_barrier();
        t2_ = *(const u32*)((li < 16 ? wa : wu) + (li & 15) * 1024 + c16 * 64);
        asm volatile("" :: "v"(t0_), "v"(t1_), "v"(t2_));
        asm volatile("s_waitcnt vmcnt(0)" ::: "memory");
        u32 best0 = ~0u, best1 = ~0u;
#pragma unroll
        for (int i = 0; i < 3; ++i) {
            u64 t0, t1, t2; u32 v;
            asm volatile("s_memtime %0\n\ts_waitcnt lgkmcnt(0)\n\ts_load_dword %3, %4, %5\n\ts_waitcnt lgkmcnt(0)\n\t"
                         "s_memtime %1\n\ts_waitcnt lgkmcnt(0)\n\ts_load_dword %3, %4, %6\n\ts_waitcnt lgkmcnt(0)\n\t"
                         "s_memtime %2\n\ts_waitcnt lgkmcnt(0)"
                         : "=&s"(t0), "=&s"(t1), "=&s"(t2), "=&s"(v) : "s"(pa), "i"(i * 1152), "i"(8192 + i * 1152) : "memory");
            best0 = min(best0, (u32)(t1 - t0)); best1 = min(best1, (u32)(t2 - t1));
        }
        const int d = max(-400, min(400, (int)best0 - (int)best1));
        if (lane == 0) st_word(tab + POL_OFF + bucket * 8, (u32)d, __builtin_amdgcn_readfirstlane(tag) + 1u);
        ST(6);
    }
    asm volatile("" : "+v"(tag) :: "memory");
    tag = __builtin_amdgcn_readfirstlane(tag) + 1u;

    const bool gemm = w < 12, second = extra && w == (li & 3);
    u32x2 td[8];
    const auto rs = __builtin_amdgcn_make_buffer_rsrc((void*)tab, 0, TAB_BYTES, 0x00020000);
    auto put_rows = [&](int tile, int lo = 0, int hi = 16) {
#pragma unroll
        for (int i = 0; i < 16; ++i) if (i >= lo && i < hi) {
            const u32 voff = h * 2048 + m * 128 + r * 4 + (i & 3) * 512, unit = (u32)tile * 2 + (i >> 3), sb = unit * 16384 + ((i >> 2) & 1) * 4096;
            if (once_unit(unit)) __builtin_amdgcn_raw_buffer_store_b32(td[i >> 1][i & 1], rs, voff, sb + side_of(unit, 0u) * 8192, 16);
            else { __builtin_amdgcn_raw_buffer_store_b32(td[i >> 1][i & 1], rs, voff, sb, 16); __builtin_amdgcn_raw_buffer_store_b32(td[i >> 1][i & 1], rs, voff, sb + 8192, 16); }
        }
    };
    auto table_tile = [&](u32 twad, u32 txa0, u32 txa1) {
        bf16x8 fb[8], fv[8], xf[8];
        f32x16 ab = {}, av = {};
#pragma unroll
        for (int s = 0; s < 11; ++s) {
            if (s < 8) {
                xf[s] = lds_ld<bf16x8>(((s & 1) ? txa1 : txa0) + (s >> 1) * 64);
                fb[s] = lds_tr2(twad + s * 4096, twad + s * 4096 + 1024); fv[s] = lds_tr2(twad + 32768 + s * 4096, twad + 32768 + s * 4096 + 1024);
            }
            if (s >= 3) { ab = MFMA(xf[s - 3], fb[s - 3], ab); av = MFMA(xf[s - 3], fv[s - 3], av); }
            __builtin_amdgcn_sched_barrier(0);
        }
        float vmax = 0.f;
#pragma unroll
        for (int i = 0; i < 16; i += 2) {
            vmax = fmaxf(fmaxf(vmax, fabsf(av[i])), fabsf(av[i + 1]));
            const u32 vv = pk_f16(av[i], av[i + 1]), vn = vv ^ 0x80008000u;
            const u32 b0 = __float_as_uint(solo(__builtin_fmaf(ab[i], -LOG2E, 0x1p-100f))) - (u32)(int)(short)vn;
            const u32 b1 = __float_as_uint(solo(__builtin_fmaf(ab[i + 1], -LOG2E, 0x1p-100f))) - (u32)((int)vn >> 16);
            td[i >> 1] = u32x2{__builtin_amdgcn_perm(b0, vv, 0x07060100u), __builtin_amdgcn_perm(b1, vv, 0x07060302u)};
            asm volatile("" : "+v"(td[i >> 1]));
        }
        const u32 fl = (__any(!(vmax <= 60000.f)) ? 1u : 0u) | (__any(vmax >= 0x1p-10f) ? 2u : 0u) | (__any(vmax > 0.f) ? 4u : 0u);
        lds_one(sm + C_VFL, fl, true);
        lds_post(sm + C_FRAG, lane);
    };
    if (gemm) {
        const u32 twad = opaque(sm + L_W + w_addr(lane, m)), xb = sm + L_X + (g * 8 + (r >> 2)) * XP + (r & 3) * 256;
        const u32 txa0 = opaque(xb + ((h ^ (r & 3)) << 4)), txa1 = opaque(xb + (((2 + h) ^ (r & 3)) << 4));
        asm volatile("" :: "v"(twad), "v"(txa0), "v"(txa1));
        if (g == 1) { asm volatile("s_waitcnt vmcnt(0)" ::: "memory"); lds_post(sm + C_DMA1, lane); }
        lds_wait(sm + C_DMA, 8u);
        if (w >= 8) lds_wait(sm + C_DMA1, 4u);
        ST(1);
        if (w < 4) __builtin_amdgcn_s_setprio(2); else if (w < 8) __builtin_amdgcn_s_setprio(1); else __builtin_amdgcn_s_setprio(0);
#pragma unroll 1
        for (int pass = 0;; ++pass) {
            table_tile(twad, opaque(txa0 + pass * 25 * XP), opaque(txa1 + pass * 25 * XP));
            if (pass || w >= 8) break;
            if (w >= 4 || A_GATE) lds_wait(sm + C_OUT, 2u);
            put_rows(g * 256 + bucket);
            __builtin_amdgcn_s_setprio(0);
            if (!second) break;
            lds_wait(sm + C_W1, 3u);
        }
        ST(2);
    }

    const int gs = (g * 49 + 1) >> 1;
    auto x_addr = [&](int ln, int odd) { const int rr = min(gs + (ln & 31), 99); return sm + L_X + (rr >> 2) * XP + (rr & 3) * 256 + (((2 * odd + (ln >> 5)) ^ (rr & 3)) << 4); };
    auto x_tile = [&](int ln) {
        const u32 ia = sm + L_LUT + (ln & 31) * LP + (ln >> 5) * 16;
        const f32x16 t = MFMA(lds_ld<bf16x8>(x_addr(ln, 0) + m * 64), lds_ld<bf16x8>(ia), f32x16{});
        return MFMA(lds_ld<bf16x8>(x_addr(ln, 1) + m * 64), lds_ld<bf16x8>(ia + 32), t);
    };
    u32x8 af; u32x4 wcf; f32x16 num = {}, den, ux;
    auto node_mm = [&](u32 wimg, f32x16 acc) {
        const u32 wad = opaque(wimg + w_addr(lane, m)), xa0 = opaque(x_addr(lane, 0)), xa1 = opaque(x_addr(lane, 1));
        bf16x8 xf[8], wf[8];
#pragma unroll
        for (int s = 0; s < 8; ++s) {
            xf[s] = lds_ld<bf16x8>(((s & 1) ? xa1 : xa0) + (s >> 1) * 64); wf[s] = lds_tr2(wad + s * 4096, wad + s * 4096 + 1024);
            if (s >= 4) acc = MFMA(xf[s - 4], wf[s - 4], acc);
            __builtin_amdgcn_sched_barrier(0);
        }
#pragma unroll
        for (int s = 4; s < 8; ++s) acc = MFMA(xf[s], wf[s], acc);
        return acc;
    };
    auto part_u = [&]() { ux = node_mm(sm + (ORD ? L_W1 : L_W), x_tile(lane)); asm volatile("" : "+v"(ux)); };
    auto part_a = [&]() {
        const f32x16 aa = node_mm(sm + (ORD ? L_W : L_W1), f32x16{});
        const u32 wo = sm + L_WC + w_addr(lane, m);
        wcf = __builtin_bit_cast(u32x4, lds_tr2(wo, wo + 1024));
        float amax = 0.f;
#pragma unroll
        for (int i = 0; i < 14; i += 2) {
            amax = fmaxf(fmaxf(amax, fabsf(aa[i])), fabsf(aa[i + 1]));
            af[i >> 1] = pk_bf16(__builtin_amdgcn_exp2f(solo(aa[i] * LOG2E)), __builtin_amdgcn_exp2f(solo(aa[i + 1] * LOG2E)));
            den[i] = __builtin_amdgcn_exp2f(solo(__builtin_fmaf(aa[i], -LOG2E, -19.931568569324174f)));
            den[i + 1] = __builtin_amdgcn_exp2f(solo(__builtin_fmaf(aa[i + 1], -LOG2E, -19.931568569324174f)));
        }
        af[7] = 0u; den[14] = 0.f; den[15] = 0.f;
        if (__any(!(amax <= 44.f))) lds_one(sm + C_FLAG, 1u, true);
#pragma unroll
        for (int i = 0; i < 4; ++i) wcf[i] = pk_bf16(solo(bf_lo(wcf[i]) * -LOG2E), solo(bf_hi(wcf[i]) * -LOG2E));
        asm volatile("" : "+v"(af), "+v"(wcf), "+v"(den));
    };
    auto drained = [&](int stage) {
        u32 old;
        asm volatile("s_mov_b64 exec, 1\n\tds_add_rtn_u32 %0, %1, %2\n\ts_mov_b64 exec, -1\n\ts_waitcnt lgkmcnt(0)" : "=&v"(old) : "v"(sm + (stage ? C_DRAIN1 : C_DRAIN)), "v"(1u) : "memory");
        if (__builtin_amdgcn_readfirstlane(old) == (stage ? ngemm - 9u : 7u) && lane < 8)
            st_word(tab + DON_OFF + stage * 16384 + (lane * 256 + bucket) * 8, lds_ld<u32>(sm + C_VFL), tag);
    };
    const auto re = __builtin_amdgcn_make_buffer_rsrc((void*)ea, 0, NE * 32, 0x00020000);
    const u32 mk = sm + L_MK + m * 4096;
    if (g == 3) {
        __builtin_amdgcn_s_setprio(3);
        const int cell = m * 64 + lane;
        lds_wait(sm + C_OUT, 2u);
        lds_st<u32x4>(mk + lane * 32, z4); lds_st<u32x4>(mk + lane * 32 + 16, z4);
        const char* pc = tab + OFF_OFF + (min(cell, NCH - 1) * 256 + bucket) * 8;
        if (D_DELAY) lds_wait(sm + C_FRAG, D_DELAY);
        u32x2 v = ld_word(pc);
        bool dead = false;
        for (u32 spin = 0; !__all(v[1] == tag); ++spin) {
            if (spin > SPIN_CAP) { dead = true; break; }
            __builtin_amdgcn_s_sleep(POLL_SLEEP);
            if (v[1] != tag) v = ld_word(pc);
        }
        ST(13);
        if (W2_EARLY) lds_post(sm + C_TRA, lane);
        const u32 cnt = (cell < NCH && !dead) ? (v[0] >> 16) : 0u;
        u32 inc = row_scan(cnt);
        inc += __builtin_amdgcn_update_dpp(0u, inc, 0x142, 0xa, 0xf, true); inc += __builtin_amdgcn_update_dpp(0u, inc, 0x143, 0xc, 0xf, true);
        const u32 nw = __builtin_amdgcn_readlane(inc, 63);
        if (cnt && inc - cnt < 512u) lds_st<u32>(mk + (inc - cnt) * 4, (u32)cell * CH + (v[0] & 0xffffu) - (inc - cnt));
        asm volatile("s_waitcnt lgkmcnt(0)" ::: "memory");
        u32x4 a = lds_ld<u32x4>(mk + lane * 32), b = lds_ld<u32x4>(mk + lane * 32 + 16);
        const u32 top = max(max(max(a[0], a[1]), max(a[2], a[3])), max(max(b[0], b[1]), max(b[2], b[3])));
        a[0] = max(a[0], (u32)__builtin_amdgcn_update_dpp(0u, wave_max_scan(top), 0x138, 0xf, 0xf, true));
        a[1] = max(a[1], a[0]); a[2] = max(a[2], a[1]); a[3] = max(a[3], a[2]);
        b[0] = max(b[0], a[3]); b[1] = max(b[1], b[0]); b[2] = max(b[2], b[1]); b[3] = max(b[3], b[2]);
        lds_st<u32x4>(mk + lane * 32, a); lds_st<u32x4>(mk + lane * 32 + 16, b);
        asm volatile("s_waitcnt lgkmcnt(0)" ::: "memory");
        const auto rrec = __builtin_amdgcn_make_buffer_rsrc((void*)(tab + REC_OFF), 0, NE * 8, 0x00020000);
        u32 ix[8];
#pragma unroll
        for (int k = 0; k < 8; ++k) ix[k] = lds_ld<u32>(mk + lane * 4 + k * 256);
#pragma unroll
        for (int k = 0; k < 8; ++k) td[k] = __builtin_amdgcn_raw_buffer_load_b64(rrec, (u32)(lane + 64 * k) < nw ? (ix[k] + lane) * 8u : 0x7ffff000u, k * 512, 16);
        lds_post(sm + C_GO, lane);
        lds_st<u32>(sm + L_NW + m * 4, nw);
        if (nw > 512u || dead) lds_one(sm + C_FLAG, dead ? 8u : 1u, true);
        lds_wait(sm + C_DMA, 8u);
    }
    lds_wait(sm + C_DMA1, 4u); lds_wait(sm + C_W1, 3u);
    if (ORD) part_u(); else part_a();
    ST(3);
    if (g == 3) {
#pragma unroll
        for (int k = 0; k < 8; ++k) lds_st<u32x2>(mk + lane * 8 + k * 512, td[k]);
        __builtin_amdgcn_s_setprio(0);
    } else if (g < 2) {
        lds_wait(sm + C_FRAG, ngemm);
        asm volatile("s_waitcnt vmcnt(0)" ::: "memory");
        drained(0);
        if (w == 4) {
            const char* pp = tab + POL_OFF + (cls * 32 + r) * 8;
            u32x2 pv = ld_word(pp);
            for (u32 spin = 0; !__all(pv[1] == tag) && spin <= SPIN_CAP; ++spin) { __builtin_amdgcn_s_sleep(POLL_SLEEP); pv = ld_word(pp); }
            lds_st<u32>(sm + L_POL, (u32)__ballot((int)pv[0] + __builtin_amdgcn_ds_swizzle((int)pv[0], 0x401f) > 0));
        }
        const u16* wp = (ORD ? wa : wu) + (w * 16 + q) * 128 + ((c16 ^ (q << 2)) << 3);
        lds_wait(sm + (W2_EARLY ? C_TRA : C_GO), W2_EARLY ? W2_EARLY : 4u);
        glds16x4(wp, sm + L_W + w * 4096);
        if (second && W2_EARLY) lds_wait(sm + C_GO, 4u);
        if (second) put_rows(768 + cls * 2 + (li >> 2));
    } else {
        lds_wait(sm + C_GO, 4u);
        put_rows(512 + bucket, 0, C_CUT);
    }
    ST(9);
    wg_barrier();
    ST(14);
    int hl, nb; bool early, anyearly;
    {
        const u32 nw = lds_ld<u32>(sm + L_NW + m * 4), polmask = lds_ld<u32>(sm + L_POL), i0 = g * 64 + lane;
        const u32x2 r0 = lds_ld<u32x2>(mk + i0 * 8), r1 = lds_ld<u32x2>(mk + i0 * 8 + 2048);
        const bool a0 = i0 < nw, a1 = i0 + 256u < nw;
        u32 k0 = 0, k1 = 0;
        if (a0) k0 = lds_add(sm + L_HIST + (r0[1] >> 24) * 4u, 1u);
        if (a1) k1 = lds_add(sm + L_HIST + (r1[1] >> 24) * 4u, 1u);
        if (g == 2) put_rows(512 + bucket, C_CUT, C_CUT2);
        const u32 t0 = ((r0[0] & TMASK) << 4) | (side_of((r0[0] & 0xffffu) >> 5, polmask) << 8);
        const u32 t1 = ((r1[0] & TMASK) << 4) | (side_of((r1[0] & 0xffffu) >> 5, polmask) << 8);
        asm volatile("" :: "v"(t0), "v"(t1));
        wg_barrier();
        ST(16);
        if (g == 2) put_rows(512 + bucket, C_CUT2, 16);
        const u32x2 c = lds_ld<u32x2>(sm + L_HIST + lane * 8);
        const u32 inc = row_scan(c[0] + c[1]);
        const u32 b0 = (lane >> 4) * 512 + inc - c[0] - c[1];
        lds_st<u32x2>(sm + L_BASE + lane * 8, u32x2{b0, b0 + c[0]});
        const u32 ng = __builtin_amdgcn_readlane(inc, g * 16 + 15);
        hl = (int)((ng + 15u) >> 4);
        int na = max(__builtin_amdgcn_readlane(inc, 15), __builtin_amdgcn_readlane(inc, 31)), nc = max(__builtin_amdgcn_readlane(inc, 47), __builtin_amdgcn_readlane(inc, 63));
        asm("" : "+s"(na), "+s"(nc));
        nb = (max(na, nc) - 1) >> 6;
        anyearly = (__ballot(dpp0<0x116>(inc) < min(inc, 160u)) & 0x8000800080008000ull) != 0;
        early = (u32)__builtin_amdgcn_readlane(inc, g * 16 + 9) < min(ng, 160u);
        asm volatile("s_waitcnt lgkmcnt(0)" ::: "memory");
        k0 += lds_ld<u32>(sm + L_BASE + (r0[1] >> 24) * 4u); k1 += lds_ld<u32>(sm + L_BASE + (r1[1] >> 24) * 4u);
        auto place = [&](u32 t, u32x2 v, u32 p) { lds_st<u32>(sm + L_LT + p * 4u, t); lds_st<u32x2>(sm + L_LR + p * 8u, v); };
        if (!__any(inc > 512u)) {
            if (a0) place(t0, r0, k0);
            if (a1) place(t1, r1, k1);
            if (m == 0 && (u32)lane < ((0u - ng) & 31u)) place(0u, u32x2{(32u * LP) << 16, 0u}, g * 512 + ng + lane);
        } else lds_one(sm + C_FLAG, 1u, true);
    }
    const bool post_late = LATE_POST && !anyearly;
    if (g < 2 || !post_late) asm volatile("s_waitcnt vmcnt(0)" ::: "memory");
    if (second || (g == 2 && !post_late)) drained(1);
    auto await = [&](int stage) {
        const char* p = tab + DON_OFF + stage * 16384 + (cls * 256 + lane) * 8;
        u32 f = 8u;
        for (u32 spin = 0; spin <= SPIN_CAP; ++spin) {
            const u32x2 v0 = ld_word(p), v1 = ld_word(p + 512), v2 = ld_word(p + 1024), v3 = ld_word(p + 1536);
            if (__all(v0[1] == tag && v1[1] == tag && v2[1] == tag && v3[1] == tag)) {
                const u32 o = v0[0] | v1[0] | v2[0] | v3[0];
                f = (__any(o & 1u) || (__any(o & 4u) && !__any(o & 2u))) ? 1u : 0u;
                break;
            }
            __builtin_amdgcn_s_sleep(POLL_SLEEP);
        }
        lds_one(sm + C_FLAG, f, true);
    };
    if (w == 1 || (m == 0 && early)) await(w == 1 ? 0 : 1);
    if (j == 0 && tid == 64) __hip_atomic_store((GAS u32*)(tab + EPO_OFF), tag, __ATOMIC_RELAXED, __HIP_MEMORY_SCOPE_AGENT);
    ST(10);
    wg_barrier();
    ST(17);

    auto slot_of = [](int rho) { return (rho & 19) | ((rho & 4) << 1) | ((rho & 8) >> 1); };
    const auto rt = __builtin_amdgcn_make_buffer_rsrc((void*)(tab + m * 128), (short)0x8000, 0x7fffffff, 0x00c00000);
    const int er = 8 * m + (lane >> 3);
    u32x4 ptr = {sm + L_LT + g * 2048 + h * 32, sm + L_LR + g * 4096 + slot_of(r) * 8,
                 sm + L_LR + g * 4096 + (4 * h + ((lane >> 2) & 3)) * 8, sm + L_LR + g * 4096 + slot_of(er) * 8 + 4};
    const u32x4 lc = {sm + L_E + g * 4096 + (2 * r + (h ^ ((r >> 3) & 1))) * 16,
                      sm + L_E + g * 4096 + (2 * er + (((lane >> 2) & 1) ^ (m & 1))) * 16 + (lane & 3) * 4,
                      sm + L_LUT + h * 16, sm + L_LUT + ((lane >> 4) & 1) * 32 + (lane & 3) * 8};
    const u32x2 lc2 = {(u32)(lane & 7) * 4, 0xddddu};
    if (__builtin_amdgcn_readfirstlane(lds_ld<u32>(sm + C_FLAG))) hl = 0;
    f32x16 dA; u32x2 ee, e01;
    asm volatile("s_nop 4\n\t"
                 "ds_read2st64_b32 v[112:113], v117 offset0:0 offset1:1\n\t"
                 "ds_read2st64_b32 v[124:125], v117 offset0:2 offset1:3\n\t"
                 "ds_read_b128 v[32:35], v114\n\tds_read_b128 v[36:39], v114 offset:16\n\t"
                 "ds_read_b128 v[40:43], v114 offset:64\n\tds_read_b128 v[44:47], v114 offset:80\n\t"
                 "s_cmp_lt_i32 %[hl], 1\n\t"
                 "s_waitcnt lgkmcnt(0)\n\t"
                 "s_cbranch_scc1 LF%=\n\t"
                 "v_and_or_b32 v112, v112, %[em], v122\n\tv_and_or_b32 v113, v113, %[em], v122\n\t"
                 "v_and_or_b32 v124, v124, %[em], v122\n\tv_and_or_b32 v125, v125, %[em], v122\n\t"
                 "buffer_load_dword v112, v112, %[re], 0 offen\n\tbuffer_load_dword v113, v113, %[re], 0 offen\n\t"
                 "s_cmp_lt_u32 %[g], " XS(FIRST_G) "\n\ts_cbranch_scc0 LF%=\n\t"
                 G8(I_LD, 32, 0) G8(I_LD, 32, 8)
                 "LF%=:"
                 : "={v[32:47]}"(dA), "={v[124:125]}"(ee), "={v[112:113]}"(e01)
                 : "{v[114:117]}"(ptr), "{v[122:123]}"(lc2), [rt] "s"(rt), [re] "s"(re), [em] "s"(EMASK), [hl] "s"(hl), [g] "s"(g) : "memory", "scc");
    if (ORD) part_a(); else part_u();
    ST(15);
    asm volatile("s_cmp_lt_u32 %[g], " XS(FIRST_G) "\n\ts_cbranch_scc1 LW%=\n\ts_waitcnt vmcnt(0)\nLW%=:\n\ts_waitcnt vmcnt(16)"
                 : "+{v[112:113]}"(e01) : [g] "s"(g) : "memory", "scc");
    if (g == 2 && post_late) { if (FIRST_G > 2 && hl < 1) asm volatile("s_waitcnt vmcnt(0)" ::: "memory"); drained(1); }
    lds_st<u32>(lc[1], e01[0]); lds_st<u32>(lc[1] + 1024, e01[1]);
    wg_barrier();
    const u32 fl = __builtin_amdgcn_readfirstlane(lds_ld<u32>(sm + C_FLAG));
    if (fl) hl = 0;
    ux[13] = __uint_as_float(x_addr(lane, 0) + m * 64); ux[14] = __uint_as_float(x_addr(lane, 1) + m * 64); ux[15] = __uint_as_float(sm + L_LUT + r * LP + h * 16);
    u32 pk = sm + L_W + w * 4096 + lane * 16, sp = SPIN_CAP;
    const int late = g < FIRST_G ? 0 : hl;
    const u32 dq = (u32)(m * 64 + lane) * 8u + 4u;
    const auto rd = __builtin_amdgcn_make_buffer_rsrc((void*)(tab + DON_OFF + 16384 + cls * 2048), 0, 2048, 0x00020000);
    ST(18);

    asm volatile("s_waitcnt lgkmcnt(0)\n\ts_nop 4\n\t"
                 "s_cmp_lt_i32 %[k], 1\n\ts_cbranch_scc1 LG%=\n\t"
                 G8(I_LD, 32, 0) G8(I_LD, 32, 8)
                 "LG%=:\n\t"
                 "ds_write_b128 v113, v[48:51]\n\tds_write_b128 v113, v[52:55] offset:1024\n\t"
                 "ds_write_b128 v113, v[56:59] offset:2048\n\tds_write_b128 v113, v[60:63] offset:3072\n\t"
                 "s_cmp_lt_i32 %[hl], 1\n\t"
                 "s_waitcnt lgkmcnt(0)\n\t"
                 "s_cbranch_scc1 LEND%=\n\t"
                 "buffer_load_dword v124, v124, %[re], 0 offen\n\tbuffer_load_dword v125, v125, %[re], 0 offen\n\t"
                 "s_setprio 3\n"
                 "LTOP%=:\n\t"
                 STEP(32, 48, 0, "LA", 2, "s_setprio 2\n\t", MID_EVEN)
                 STEP(48, 32, 1, "LB", 0, "s_setprio 0\n\t", MID_ODD(1, "LB", ""))
                 STEP(32, 48, 2, "LC", 2, "s_setprio 2\n\t", MID_EVEN)
                 STEP(48, 32, 3, "LD", 0,
                      "s_setprio 0\n\ts_cmp_eq_u32 %[sp], 0\n\ts_cbranch_scc1 LQ%=\n\tbuffer_load_dword v127, v126, %[rd], 0 offen sc1\nLQ%=:\n\t",
                      MID_ODD(3, "LD", "s_cmp_eq_u32 %[sp], 0\n\ts_cbranch_scc1 LS%=\n\t"
                      "s_cmp_lt_i32 %[hl], 1\n\ts_cbranch_scc1 LS%=\n\ts_waitcnt vmcnt(16)\n"
                      "LR%=:\n\t"
                      "v_cmp_ne_u32 vcc, %[tag], v127\n\ts_sub_u32 %[sp], %[sp], 1\n\ts_nop 3\n\ts_cbranch_vccz LT%=\n\t"
                      "s_cmp_eq_u32 %[sp], 0\n\ts_cbranch_scc1 LT%=\n\t"
                      "s_sleep 4\n\tbuffer_load_dword v127, v126, %[rd], 0 offen sc1\n\ts_waitcnt vmcnt(0)\n\ts_branch LR%=\n"
                      "LT%=:\n\ts_mov_b32 %[sp], 0\n"
                      "LS%=:\n\t"))
                 "v_add_u32 v114, 0x200, v114\n\tv_add_u32 v115, 0x400, v115\n\tv_add_u32 v116, 0x400, v116\n\tv_add_u32 v117, 0x400, v117\n\t"
                 "s_branch LTOP%=\n"
                 "LEND%=:\n\t"
                 "s_waitcnt vmcnt(0)\n\t"
                 "s_setprio 3\n\ts_cmp_gt_i32 %[nb], 0\n\ts_cbranch_scc1 LX%=\n\ts_setprio 0\nLX%=:\n\ts_nop 15"
                 : "+{v[0:15]}"(num), "+{v[16:31]}"(den), "+{v[32:47]}"(dA), "+{v[48:63]}"(ux), "+{v[124:125]}"(ee), "+{v113}"(pk), "+{v[114:117]}"(ptr), [hl] "+s"(hl), [sp] "+s"(sp), [nb] "+s"(nb)
                 : "{v[80:87]}"(af), "{v[88:91]}"(wcf), "{v[118:121]}"(lc), "{v[122:123]}"(lc2), [rt] "s"(rt), [re] "s"(re), [em] "s"(EMASK),
                   "{v126}"(dq), [rd] "s"(rd), [tag] "s"(tag), [k] "s"(late)
                 : "memory", "scc", "vcc", "v127",
                   "v64", "v65", "v66", "v67", "v68", "v69", "v70", "v71", "v72", "v73", "v74", "v75", "v76", "v77", "v78", "v79",
                   "v92", "v93", "v94", "v95", "v96", "v97", "v98", "v99", "v100", "v101", "v102", "v103", "v104", "v105", "v106", "v107",
                   "v108", "v109", "v110", "v111", "v112");
    if (fl) { if (!(fl & 8u)) slow_bucket(x, ea, src, dst, wa, wb, wc, wu, wv, tab, out, bucket, sm, w); return; }
    ST(19);
    {
        const int nrow = max(min(25 - (g & 1), NN - node0 - gs), 0);
        const auto ro = __builtin_amdgcn_make_buffer_rsrc((void*)(out + min(node0 + gs, NN - 1) * 128), 0, nrow * 256, 0x00020000);
        asm volatile("s_waitcnt lgkmcnt(0)\n\t"
                     "v_mbcnt_lo_u32_b32 v64, -1, 0\n\tv_mbcnt_hi_u32_b32 v64, -1, v64\n\tv_lshl_add_u32 v65, v64, 4, %[own]\n\t"
                     "ds_read_b128 v[44:47], v65 offset:3072\n\tds_read_b128 v[32:35], v65\n\t"
                     "ds_read_b128 v[36:39], v65 offset:1024\n\tds_read_b128 v[40:43], v65 offset:2048\n\t"
                     "v_lshrrev_b32 v66, 5, v64\n\tv_and_b32 v64, 31, v64\n\tv_lshl_add_u32 v64, v64, 1, %[mo]\n\t"
                     "v_lshl_add_u32 v66, v66, 10, v64\n\tv_add_u32 v67, 0x1000, v66\n\t"
                     "s_waitcnt lgkmcnt(3)\n\t"
                     "ds_read_b128 v[68:71], v45\n\tds_read_b128 v[72:75], v47\n\tds_read_b128 v[76:79], v46\n\tds_read_b128 v[80:83], v47 offset:32\n\t"
                     "v_rcp_f32 v16, v16\n\tv_rcp_f32 v17, v17\n\tv_rcp_f32 v18, v18\n\tv_rcp_f32 v19, v19\n\tv_rcp_f32 v20, v20\n\tv_rcp_f32 v21, v21\n\tv_rcp_f32 v22, v22\n\tv_rcp_f32 v23, v23\n\tv_rcp_f32 v24, v24\n\tv_rcp_f32 v25, v25\n\tv_rcp_f32 v26, v26\n\tv_rcp_f32 v27, v27\n\tv_rcp_f32 v28, v28\n\t"
                     "s_waitcnt lgkmcnt(0)\n\t"
                     "v_mfma_f32_32x32x16_bf16 v[48:63], v[68:71], v[72:75], 0\n\tv_mfma_f32_32x32x16_bf16 v[48:63], v[76:79], v[80:83], v[48:63]\n\t"
                     "v_fmac_f32 v32, v0, v16\n\tv_fmac_f32 v33, v1, v17\n\tv_fmac_f32 v34, v2, v18\n\tv_fmac_f32 v35, v3, v19\n\tv_fmac_f32 v36, v4, v20\n\tv_fmac_f32 v37, v5, v21\n\tv_fmac_f32 v38, v6, v22\n\tv_fmac_f32 v39, v7, v23\n\tv_fmac_f32 v40, v8, v24\n\tv_fmac_f32 v41, v9, v25\n\tv_fmac_f32 v42, v10, v26\n\tv_fmac_f32 v43, v11, v27\n\tv_fmac_f32 v44, v12, v28\n\t"
                     "v_max_f32 v32, v32, v48\n\tv_max_f32 v33, v33, v49\n\tv_max_f32 v34, v34, v50\n\tv_max_f32 v35, v35, v51\n\tv_max_f32 v36, v36, v52\n\tv_max_f32 v37, v37, v53\n\tv_max_f32 v38, v38, v54\n\tv_max_f32 v39, v39, v55\n\tv_max_f32 v40, v40, v56\n\tv_max_f32 v41, v41, v57\n\tv_max_f32 v42, v42, v58\n\tv_max_f32 v43, v43, v59\n\tv_max_f32 v44, v44, v60\n\t"
                     "v_cvt_pk_bf16_f32 v32, v32, v33\n\tv_cvt_pk_bf16_f32 v34, v34, v35\n\tv_cvt_pk_bf16_f32 v36, v36, v37\n\tv_cvt_pk_bf16_f32 v38, v38, v39\n\tv_cvt_pk_bf16_f32 v40, v40, v41\n\tv_cvt_pk_bf16_f32 v42, v42, v43\n\tv_cvt_pk_bf16_f32 v44, v44, v44\n\t"
                     "buffer_store_short v32, v66, %[ro], 0 offen offset:0" OUT_POL "\n\t"
                     "buffer_store_short_d16_hi v32, v66, %[ro], 0 offen offset:256" OUT_POL "\n\t"
                     "buffer_store_short v34, v66, %[ro], 0 offen offset:512" OUT_POL "\n\t"
                     "buffer_store_short_d16_hi v34, v66, %[ro], 0 offen offset:768" OUT_POL "\n\t"
                     "buffer_store_short v36, v66, %[ro], 0 offen offset:2048" OUT_POL "\n\t"
                     "buffer_store_short_d16_hi v36, v66, %[ro], 0 offen offset:2304" OUT_POL "\n\t"
                     "buffer_store_short v38, v66, %[ro], 0 offen offset:2560" OUT_POL "\n\t"
                     "buffer_store_short_d16_hi v38, v66, %[ro], 0 offen offset:2816" OUT_POL "\n\t"
                     "buffer_store_short v40, v67, %[ro], 0 offen offset:0" OUT_POL "\n\t"
                     "buffer_store_short_d16_hi v40, v67, %[ro], 0 offen offset:256" OUT_POL "\n\t"
                     "buffer_store_short v42, v67, %[ro], 0 offen offset:512" OUT_POL "\n\t"
                     "buffer_store_short_d16_hi v42, v67, %[ro], 0 offen offset:768" OUT_POL "\n\t"
                     "buffer_store_short v44, v67, %[ro], 0 offen offset:2048" OUT_POL "\n\t"
                     : "+{v[16:31]}"(den) : "{v[0:15]}"(num), [own] "s"(sm + L_W + w * 4096), [mo] "s"(m * 64), [ro] "s"(ro)
                     : "memory", "v32", "v33", "v34", "v35", "v36", "v37", "v38", "v39", "v40", "v41", "v42", "v43", "v44", "v45", "v46", "v47", "v48", "v49", "v50", "v51", "v52", "v53", "v54", "v55", "v56", "v57", "v58", "v59", "v60", "v61", "v62", "v63", "v64", "v65", "v66", "v67", "v68", "v69", "v70", "v71", "v72", "v73", "v74", "v75", "v76", "v77", "v78", "v79", "v80", "v81", "v82", "v83");
    }
    ST(21); ST_OUT(0);
}

extern "C" void kernel_launch(void* const* d_in, const int* in_sizes, int n_in, void* d_out, int out_size, void* d_ws, size_t ws_size,
                              hipStream_t stream) {
    gatedgcn<<<256, 1024, 0, stream>>>((char*)d_ws, (const u16*)d_in[0], (const int*)d_in[2], (const int*)d_in[3], (const u16*)d_in[5], (const u16*)d_in[8],
                                       (const u16*)d_in[6], (const u16*)d_in[4], (const u16*)d_in[7], (const u16*)d_in[1], (u16*)d_out);
}
```
